```python
import torch

def _softmax(x, axis=-1):
    return torch.softmax(x, dim=axis)
def _gelu(x):
    return torch.nn.functional.gelu(x, approximate="tanh")
def _silu(x):
    return torch.nn.functional.silu(x)
def _sigmoid(x):
    return torch.sigmoid(x)
def _layernorm(x, axis=-1, eps=1e-5):
    m = x.mean(dim=axis, keepdim=True)
    v = x.var(dim=axis, keepdim=True, unbiased=False)
    return (x - m) / torch.sqrt(v + eps)
def _scatter_add(tgt, idx, src, axis=0):
    return tgt.clone().index_add_(axis, idx, src)
def _segment_sum(src, seg, n):
    out = torch.zeros((n,) + src.shape[1:], dtype=src.dtype)
    return out.index_add_(0, seg, src)
def _pad(x, pw):
    flat = tuple(v for pair in reversed(pw) for v in pair)
    return torch.nn.functional.pad(x, flat)

kernel_name = 'sequential_preLN__mha_block_cached__glu_gated_bilinear__decode'

def setup_inputs():

    _w = (lambda a: (lambda t: t.to(torch.bfloat16) if t.is_floating_point() else t)(torch.as_tensor(a)))
    def _A_setup():
        B, Sq, D = 2, 1, 448
        H, Sk, Dh = 16, 8192, 256
        return {
            "x": _w((lambda *s: torch.randn(*s, dtype=torch.bfloat16) * 0.05)(B, Sq, D)),
            "k_cache": _w((lambda *s: torch.randn(*s, dtype=torch.bfloat16) * 1.6)(B, H, Sk, Dh)),
            "v_cache": _w((lambda *s: torch.randn(*s, dtype=torch.bfloat16) * 0.8)(B, H, Sk, Dh)),
            "wq": _w((lambda *s: torch.randn(*s, dtype=torch.bfloat16) * 1.6)(D, H * Dh) * (D ** -0.5)),
            "wo": _w((lambda *s: torch.randn(*s, dtype=torch.bfloat16) * 0.8)(H * Dh, D) * ((H * Dh) ** -0.5)),
        }
    _A_in = _A_setup()

    def _B_setup():
        B, S, D, F = 2, 1, 448, 1792
        return {
            "x": _w((lambda *s: torch.randn(*s, dtype=torch.bfloat16) * 0.05)(B, S, D)),
            "w_gate": _w((lambda *s: torch.randn(*s, dtype=torch.bfloat16) * 0.4)(D, F) * (D ** -0.5)),
            "w_up": _w((lambda *s: torch.randn(*s, dtype=torch.bfloat16) * 0.4)(D, F) * (D ** -0.5)),
            "w_down": _w((lambda *s: torch.randn(*s, dtype=torch.bfloat16) * 0.4)(F, D) * (F ** -0.5)),
        }
    _B_in = _B_setup()

    return {
        "A_x": _A_in["x"],
        "A_k_cache": _A_in["k_cache"],
        "A_v_cache": _A_in["v_cache"],
        "A_wq": _A_in["wq"],
        "A_wo": _A_in["wo"],
        "B_x": _B_in["x"],
        "B_w_gate": _B_in["w_gate"],
        "B_w_up": _B_in["w_up"],
        "B_w_down": _B_in["w_down"],
    }

def reference(A_x, A_k_cache, A_v_cache, A_wq, A_wo, B_x, B_w_gate, B_w_up, B_w_down):
    def _A_ref(x, k_cache, v_cache, wq, wo):
        B, Sq, D = x.shape
        H, Dh = 16, 256
        q = (lambda x, *d: x.permute(*d))((x @ wq).reshape(B, Sq, H, Dh), 0, 2, 1, 3)
        scores = torch.einsum("bhqd,bhkd->bhqk", q, k_cache) * (Dh ** -0.5)
        p = _softmax(scores, axis=-1)
        o = torch.einsum("bhqk,bhkd->bhqd", p, v_cache)
        return (lambda x, *d: x.permute(*d))(o, 0, 2, 1, 3).reshape(B, Sq, H * Dh) @ wo

    def _B_ref(x, w_gate, w_up, w_down):
        g = torch.einsum("bsd,df->bsf", x, w_gate)
        u = torch.einsum("bsd,df->bsf", x, w_up)
        h = g * u
        return torch.einsum("bsf,fd->bsd", h, w_down)

    x = A_x
    h = x + _A_ref(_layernorm(x, axis=-1), A_k_cache, A_v_cache, A_wq, A_wo)
    h2 = h + _B_ref(_layernorm(h, axis=-1), B_w_gate, B_w_up, B_w_down)
    return h2
```

```cpp
#include <hip/hip_runtime.h>
#include <hip/hip_bf16.h>
#include <cstdint>

typedef unsigned v4u __attribute__((ext_vector_type(4)));
typedef __bf16 bf16x2 __attribute__((ext_vector_type(2)));
typedef unsigned short u16;

constexpr int B = 2, D = 448, H = 16, SK = 8192, DH = 256, F = 1792;
constexpr int NSPLIT = 8, KPS = SK / NSPLIT;

__device__ __forceinline__ float bf2f(u16 v) { return __uint_as_float((unsigned)v << 16); }
__device__ __forceinline__ float dot2(unsigned a, unsigned b, float c) {
  return __builtin_amdgcn_fdot2_f32_bf16(__builtin_bit_cast(bf16x2, a), __builtin_bit_cast(bf16x2, b), c, false);
}
__device__ __forceinline__ unsigned cvtpk(float lo, float hi) {
  unsigned r; asm volatile("v_cvt_pk_bf16_f32 %0, %1, %2" : "=v"(r) : "v"(lo), "v"(hi)); return r;
}
__device__ __forceinline__ float wave_sum(float v) {
  for (int m = 32; m >= 1; m >>= 1) v += __shfl_xor(v, m);
  return v;
}
__device__ __forceinline__ float wave_max(float v) {
  for (int m = 32; m >= 1; m >>= 1) v = fmaxf(v, __shfl_xor(v, m));
  return v;
}

__device__ void ln_rows(const float* src, float* dst, int tid) {
  const int w = tid >> 6, lane = tid & 63;
  if (w < B) {
    float v[7], s = 0.f;
    for (int i = 0; i < 7; ++i) { v[i] = src[w * D + lane + 64 * i]; s += v[i]; }
    const float m = wave_sum(s) * (1.f / D);
    float q = 0.f;
    for (int i = 0; i < 7; ++i) { v[i] -= m; q += v[i] * v[i]; }
    const float r = rsqrtf(wave_sum(q) * (1.f / D) + 1e-5f);
    for (int i = 0; i < 7; ++i) dst[w * D + lane + 64 * i] = v[i] * r;
  }
}

__global__ void __launch_bounds__(256) k_q(const u16* x, const u16* wq, float* q) {
  __shared__ float xs[B * D], xn[B * D], red[4][B][64];
  const int tid = threadIdx.x;
  for (int i = tid; i < B * D; i += 256) xs[i] = bf2f(x[i]);
  __syncthreads();
  ln_rows(xs, xn, tid);
  __syncthreads();
  const int col = blockIdx.x * 64 + (tid & 63), w = tid >> 6;
  float a0 = 0.f, a1 = 0.f;
  for (int k = w; k < D; k += 4) { const float wv = bf2f(wq[(size_t)k * (H * DH) + col]); a0 += xn[k] * wv; a1 += xn[D + k] * wv; }
  red[w][0][tid & 63] = a0; red[w][1][tid & 63] = a1;
  __syncthreads();
  if (tid < 128) { const int b = tid >> 6, c = tid & 63; q[b * H * DH + blockIdx.x * 64 + c] = red[0][b][c] + red[1][b][c] + red[2][b][c] + red[3][b][c]; }
}

constexpr int NW = 16;
__global__ void __launch_bounds__(NW * 64) k_attn(const u16* K, const u16* V, const float* q, float* part) {
  __shared__ float accs[NW][DH], ms[NW], ls[NW];
  const int bh = blockIdx.x / NSPLIT, s = blockIdx.x % NSPLIT;
  const int tid = threadIdx.x, lane = tid & 63, w = __builtin_amdgcn_readfirstlane(tid >> 6);
  constexpr int KPW = KPS / NW;
  const size_t base = ((size_t)bh * SK + s * KPS + w * KPW) * DH;
  const v4u* kp = (const v4u*)(K + base) + lane;
  const v4u* vp = (const v4u*)(V + base) + lane;
  unsigned qp[4];
  for (int j = 0; j < 4; ++j) qp[j] = cvtpk(q[bh * DH + (lane & 31) * 8 + 2 * j], q[bh * DH + (lane & 31) * 8 + 2 * j + 1]);
  float sc[KPW / 2];
#pragma unroll
  for (int i = 0; i < KPW / 2; ++i) {
    const v4u k = __builtin_nontemporal_load(kp + i * 64);
    float d = dot2(k.x, qp[0], 0.f); d = dot2(k.y, qp[1], d); d = dot2(k.z, qp[2], d); d = dot2(k.w, qp[3], d);
    sc[i] = d;
  }
  float m = -INFINITY;
#pragma unroll
  for (int i = 0; i < KPW / 2; ++i) {
    float d = sc[i];
    for (int o = 16; o >= 1; o >>= 1) d += __shfl_xor(d, o);
    sc[i] = d * (0.0625f * 1.4426950408889634f);
    m = fmaxf(m, sc[i]);
  }
  m = fmaxf(m, __shfl_xor(m, 32));
  float l = 0.f, acc[8] = {0, 0, 0, 0, 0, 0, 0, 0};
#pragma unroll
  for (int i = 0; i < KPW / 2; ++i) {
    const v4u v = __builtin_nontemporal_load(vp + i * 64);
    const float p = __builtin_amdgcn_exp2f(sc[i] - m);
    l += p;
    for (int j = 0; j < 4; ++j) {
      acc[2 * j] = fmaf(p, __uint_as_float(v[j] << 16), acc[2 * j]);
      acc[2 * j + 1] = fmaf(p, __uint_as_float(v[j] & 0xffff0000u), acc[2 * j + 1]);
    }
  }
  l += __shfl_xor(l, 32);
  for (int j = 0; j < 8; ++j) acc[j] += __shfl_xor(acc[j], 32);
  if (lane < 32) for (int j = 0; j < 8; ++j) accs[w][lane * 8 + j] = acc[j];
  if (lane == 0) { ms[w] = m; ls[w] = l; }
  __syncthreads();
  if (tid < DH) {
    float M = -INFINITY;
    for (int i = 0; i < NW; ++i) M = fmaxf(M, ms[i]);
    float L = 0.f, o = 0.f;
    for (int i = 0; i < NW; ++i) { const float e = __builtin_amdgcn_exp2f(ms[i] - M); L += e * ls[i]; o += e * accs[i][tid]; }
    float* p = part + (size_t)blockIdx.x * (DH + 2);
    p[tid] = o;
    if (tid == 0) { p[DH] = M; p[DH + 1] = L; }
  }
}

__global__ void __launch_bounds__(256) k_wo(const float* part, const u16* wo, float* ypart) {
  __shared__ float o[B][DH];
  const int h = blockIdx.x / 7, cg = blockIdx.x % 7, tid = threadIdx.x;
  for (int b = 0; b < B; ++b) {
    const float* p = part + (size_t)((b * H + h) * NSPLIT) * (DH + 2);
    float M = -INFINITY;
    for (int s = 0; s < NSPLIT; ++s) M = fmaxf(M, p[s * (DH + 2) + DH]);
    float L = 0.f, a = 0.f;
    for (int s = 0; s < NSPLIT; ++s) { const float e = __builtin_amdgcn_exp2f(p[s * (DH + 2) + DH] - M); L += e * p[s * (DH + 2) + DH + 1]; a += e * p[s * (DH + 2) + tid]; }
    o[b][tid] = a / L;
  }
  __syncthreads();
  __shared__ float red[4][B][64];
  const int c = cg * 64 + (tid & 63), w = tid >> 6;
  float a0 = 0.f, a1 = 0.f;
  for (int k = w; k < DH; k += 4) { const float wv = bf2f(wo[(size_t)(h * DH + k) * D + c]); a0 += o[0][k] * wv; a1 += o[1][k] * wv; }
  red[w][0][tid & 63] = a0; red[w][1][tid & 63] = a1;
  __syncthreads();
  if (tid < 128) { const int b = tid >> 6, cc = tid & 63; ypart[(h * B + b) * D + cg * 64 + cc] = red[0][b][cc] + red[1][b][cc] + red[2][b][cc] + red[3][b][cc]; }
}

__global__ void __launch_bounds__(256) k_gu(const u16* x, const float* ypart, const u16* wg, const u16* wu, float* hout, float* gu) {
  __shared__ float hs[B * D], hn[B * D], red[16][4][16];
  const int tid = threadIdx.x;
  for (int i = tid; i < B * D; i += 256) { float a = bf2f(x[i]); for (int h = 0; h < H; ++h) a += ypart[h * B * D + i]; hs[i] = a; if (blockIdx.x == 0) hout[i] = a; }
  __syncthreads();
  ln_rows(hs, hn, tid);
  __syncthreads();
  const int c = blockIdx.x * 16 + (tid & 15), w = tid >> 4;
  float g0 = 0.f, g1 = 0.f, u0 = 0.f, u1 = 0.f;
  for (int k = w; k < D; k += 16) {
    const float a = bf2f(wg[(size_t)k * F + c]), b = bf2f(wu[(size_t)k * F + c]);
    g0 += hn[k] * a; g1 += hn[D + k] * a; u0 += hn[k] * b; u1 += hn[D + k] * b;
  }
  red[w][0][tid & 15] = g0; red[w][1][tid & 15] = g1; red[w][2][tid & 15] = u0; red[w][3][tid & 15] = u1;
  __syncthreads();
  if (tid < 32) {
    const int b = tid >> 4, cc = tid & 15; float g = 0.f, u = 0.f;
    for (int i = 0; i < 16; ++i) { g += red[i][b][cc]; u += red[i][2 + b][cc]; }
    gu[b * F + blockIdx.x * 16 + cc] = g * u;
  }
}

__global__ void __launch_bounds__(1024) k_down(const float* hin, const float* gu, const u16* wd, __hip_bfloat16* out) {
  __shared__ float red[64][2][16];
  const int tid = threadIdx.x, c = blockIdx.x * 16 + (tid & 15), w = tid >> 4;
  float a0 = 0.f, a1 = 0.f;
  for (int k = w; k < F; k += 64) { const float wv = bf2f(wd[(size_t)k * D + c]); a0 += gu[k] * wv; a1 += gu[F + k] * wv; }
  red[w][0][tid & 15] = a0; red[w][1][tid & 15] = a1;
  __syncthreads();
  if (tid < 32) {
    const int b = tid >> 4, cc = tid & 15; float a = 0.f;
    for (int i = 0; i < 64; ++i) a += red[i][b][cc];
    out[b * D + blockIdx.x * 16 + cc] = __float2bfloat16(hin[b * D + blockIdx.x * 16 + cc] + a);
  }
}

extern "C" void kernel_launch(void* const* d_in, const int* in_sizes, int n_in, void* d_out, int out_size, void* d_ws, size_t ws_size, hipStream_t stream) {
  const u16* x = (const u16*)d_in[0]; const u16* K = (const u16*)d_in[1]; const u16* V = (const u16*)d_in[2];
  const u16* wq = (const u16*)d_in[3]; const u16* wo = (const u16*)d_in[4];
  const u16* wg = (const u16*)d_in[6]; const u16* wu = (const u16*)d_in[7]; const u16* wd = (const u16*)d_in[8];
  float* ws = (float*)d_ws;
  float* q = ws; float* part = ws + 8192; float* ypart = part + 256 * 258; float* hbuf = ypart + 16 * 896; float* gu = hbuf + 896;
  k_q<<<64, 256, 0, stream>>>(x, wq, q);
  k_attn<<<B * H * NSPLIT, NW * 64, 0, stream>>>(K, V, q, part);
  k_wo<<<H * 7, 256, 0, stream>>>(part, wo, ypart);
  k_gu<<<F / 16, 256, 0, stream>>>(x, ypart, wg, wu, hbuf, gu);
  k_down<<<D / 16, 1024, 0, stream>>>(hbuf, gu, wd, (__hip_bfloat16*)d_out);
}
```

```cpp
#include <hip/hip_runtime.h>
#include <cstdint>

typedef unsigned u32;
typedef unsigned long long u64;
typedef u32 v4u __attribute__((ext_vector_type(4)));
typedef u32 v2u __attribute__((ext_vector_type(2)));
typedef float v4f __attribute__((ext_vector_type(4)));
typedef float v2f __attribute__((ext_vector_type(2)));
typedef short v4s __attribute__((ext_vector_type(4)));
typedef __bf16 v2b __attribute__((ext_vector_type(2)));
typedef __bf16 v8b __attribute__((ext_vector_type(8)));
typedef __amdgpu_buffer_rsrc_t rsrc_t;
#define LAS __attribute__((address_space(3)))
#define DEV __device__ __forceinline__

#define P_NPRE 10
#define P_TAU 3700
#define P_OFF (-1800)
#define P_IL16 0
#define P_WBEG 600
#define P_WSPAN 2240
#define P_DRY 1
#define P_CODEPF 0

constexpr int D = 448, F = 1792, HD = 4096;
constexpr int NPRE = P_NPRE;
constexpr int AUX_NT = 2, AUX_SC1 = 16;
constexpr u32 OOB = 0x80000000u, EMPTY = 0xFFFFFFFFu;
constexpr int SPIN_CAP = 1 << 16;

constexpr u32 WS_EPOCH = 0, WS_FLAG = 4096, WS_Q = 8192, WS_CLR = WS_Q + 262144;
constexpr u32 WS_ML = WS_CLR, WS_O = WS_ML + 8192, WS_Y = WS_O + 131072, WS_F = WS_Y + 16384;
constexpr u32 CLR_PER_WG = 1536, WS_END = WS_CLR + 256 * CLR_PER_WG;
static_assert(WS_F + 56 * 4096 <= WS_END, "cleared region covers every inbox");

constexpr int L_WQ = 0, L_WO = L_WQ + 16384, L_WG = L_WO + 16384, L_WU = L_WG + 28672, L_WD = L_WU + 28672;
constexpr int L_XN = L_WD + 28672, L_HB = L_XN + 1792, L_Q = L_HB + 1792, L_GU = L_Q + 512, L_SC = L_GU + 128, L_ACC = L_SC + 4096;
constexpr int L_CNT = L_ACC + 4096, L_END = L_CNT + 16;

DEV rsrc_t mkrs(const void* p, u32 n) { return __builtin_amdgcn_make_buffer_rsrc((void*)p, 0, n, 0x00020000); }
template <int AUX> DEV v4u ld16(rsrc_t r, u32 v, u32 s) { return __builtin_amdgcn_raw_buffer_load_b128(r, v, s, AUX); }
template <int AUX> DEV v2u ld8(rsrc_t r, u32 v, u32 s) { return __builtin_amdgcn_raw_buffer_load_b64(r, v, s, AUX); }
template <int AUX> DEV void st16(v4u x, rsrc_t r, u32 v, u32 s) { __builtin_amdgcn_raw_buffer_store_b128(x, r, v, s, AUX); }
template <int AUX> DEV void st8(v2u x, rsrc_t r, u32 v, u32 s) { __builtin_amdgcn_raw_buffer_store_b64(x, r, v, s, AUX); }
DEV void dma16(rsrc_t r, LAS char* dst, u32 v, u32 s) { __builtin_amdgcn_raw_ptr_buffer_load_lds(r, (LAS void*)dst, 16, v, s, 0, AUX_NT); }
DEV u32 now() { return (u32)__builtin_amdgcn_s_memrealtime(); }
DEV float dot2(u32 a, u32 b, float c) { return __builtin_amdgcn_fdot2_f32_bf16(__builtin_bit_cast(v2b, a), __builtin_bit_cast(v2b, b), c, false); }
DEV u32 pk(float lo, float hi) { return __builtin_bit_cast(u32, __builtin_convertvector((v2f{lo, hi}), v2b)); }
DEV float lo16(u32 x) { return __uint_as_float(x << 16); }
DEV float hi16(u32 x) { return __uint_as_float(x & 0xffff0000u); }
DEV v4f mfma(v4u a, v4u b, v4f c) { return __builtin_amdgcn_mfma_f32_16x16x32_bf16(__builtin_bit_cast(v8b, a), __builtin_bit_cast(v8b, b), c, 0, 0, 0); }
template <int C> DEV float dpp(float x) { return __builtin_bit_cast(float, __builtin_amdgcn_mov_dpp(__builtin_bit_cast(int, x), C, 0xf, 0xf, true)); }
template <int C> DEV u32 dppu(u32 x) { return (u32)__builtin_amdgcn_mov_dpp((int)x, C, 0xf, 0xf, true); }
constexpr int X1 = 0xB1, X2 = 0x4E, X7 = 0x141, X15 = 0x140, ROR4 = 0x124, ROR8 = 0x128, SHL2 = 0x102, SHL4 = 0x104, SHL6 = 0x106;
DEV float add16(float a, float b) { auto s = __builtin_amdgcn_permlane16_swap(__float_as_uint(a), __float_as_uint(b), false, false); return __uint_as_float(s[0]) + __uint_as_float(s[1]); }
DEV float add32(float a, float b) { auto s = __builtin_amdgcn_permlane32_swap(__float_as_uint(a), __float_as_uint(b), false, false); return __uint_as_float(s[0]) + __uint_as_float(s[1]); }
DEV float mul32(float a, float b) { auto s = __builtin_amdgcn_permlane32_swap(__float_as_uint(a), __float_as_uint(b), false, false); return __uint_as_float(s[0]) * __uint_as_float(s[1]); }
DEV float max16(float a) { auto s = __builtin_amdgcn_permlane16_swap(__float_as_uint(a), __float_as_uint(a), false, false); return fmaxf(__uint_as_float(s[0]), __uint_as_float(s[1])); }
DEV float max32(float a) { auto s = __builtin_amdgcn_permlane32_swap(__float_as_uint(a), __float_as_uint(a), false, false); return fmaxf(__uint_as_float(s[0]), __uint_as_float(s[1])); }
DEV float row_sum(float x) { x += dpp<X1>(x); x += dpp<X2>(x); x += dpp<X7>(x); x += dpp<X15>(x); return x; }
DEV float wave_sum(float x) { x = row_sum(x); x = add16(x, x); return add32(x, x); }
DEV float wave_max(float x) { x = fmaxf(x, dpp<X1>(x)); x = fmaxf(x, dpp<X2>(x)); x = fmaxf(x, dpp<X7>(x)); x = fmaxf(x, dpp<X15>(x)); return max32(max16(x)); }
DEV float sel(bool c, float a, float b) { return c ? a : b; }
DEV u32 safe(u32 x) { return min(x, EMPTY - 1u); }
DEV u32 umax4(v4u v) { return max(max(v.x, v.y), max(v.z, v.w)); }
DEV v4u lr16(LAS char* p) { return *(LAS v4u*)p; }
DEV v4u trfrag(LAS char* p0, LAS char* p1) {
  const v4s a = __builtin_amdgcn_ds_read_tr16_b64_v4i16((LAS v4s*)p0), b = __builtin_amdgcn_ds_read_tr16_b64_v4i16((LAS v4s*)p1);
  const v2u x = __builtin_bit_cast(v2u, a), y = __builtin_bit_cast(v2u, b);
  return v4u{x.x, x.y, y.x, y.y};
}
DEV void bar() { asm volatile("s_waitcnt lgkmcnt(0)\n\ts_barrier" ::: "memory"); }
#define FENCE() do { asm volatile("" ::: "memory"); __builtin_amdgcn_sched_barrier(0); } while (0)

#define STAMP(i) do {} while (0)
#define STAMP_DECL do {} while (0)
#define STAMP_DUMP() do {} while (0)

struct Args { const char *x, *K, *V, *wq, *wo, *wg, *wu, *wd; char *out, *ws; };

DEV void stream_wave(const Args& a, LAS char* L, int w, int lane, u32 t0, u32 pf) {
  STAMP_DECL;
  const int blk = blockIdx.x, bh = blk >> 3, s = blk & 7;
  const rsrc_t rk = mkrs(a.K + ((size_t)bh << 22), 1u << 22), rv = mkrs(a.V + ((size_t)bh << 22), 1u << 22);
  constexpr u32 PSTR = P_IL16 ? 131072 : 16384, SSTR = P_IL16 ? 16384 : 524288;
  const u32 voff = lane * 16 + w * 4096 + s * SSTR;
  int lim = P_OFF;
  auto pace = [&]() { while ((int)((now() - t0) << 6) < lim) __builtin_amdgcn_s_sleep(1); lim += P_TAU; };
  v4u buf[NPRE][4];
#pragma unroll
  for (int t = 0; t < NPRE; ++t) {
    if (t) pace(); else lim += P_TAU;
#pragma unroll
    for (int i = 0; i < 4; ++i) {
      if (t >= 2) asm volatile("s_waitcnt vmcnt(7)" ::: "memory");
      if (t == 2 && i == 0 && w == 0) STAMP(5);
      buf[t][i] = ld16<AUX_NT>(rk, voff + i * 1024, t * PSTR); FENCE();
    }
  }
  asm volatile("" :: "v"(pf));
  if (w == 0) STAMP(1);
  bar();
  if (w == 0) STAMP(2);
  const v4u q = lr16(L + L_Q + (lane & 31) * 16);
  LAS char* sc = L + L_SC + w * 1024;
  LAS char* scw = sc + (lane >> 5) * 16 + (((lane >> 4) & 1) + ((lane >> 2) & 2)) * 4;
  const bool hi8 = lane & 8, writer = (lane & 7) == 0;
  auto qk = [&](v4u k) { return dot2(k.w, q.w, dot2(k.z, q.z, dot2(k.y, q.y, dot2(k.x, q.x, 0.f)))); };
  auto reduce = [&](float d0, float d1, float d2, float d3, int t) {
    const float e0 = add16(d0, d1), e1 = add16(d2, d3);
    const float f0 = e0 + dpp<ROR8>(e0), f1 = e1 + dpp<ROR8>(e1);
    float f = hi8 ? f1 : f0;
    f += dpp<X7>(f); f += dpp<X2>(f); f += dpp<X1>(f);
    if (writer) *(LAS float*)(scw + t * 32) = f;
  };
  auto eatk = [&](int t) { reduce(qk(buf[t][0]), qk(buf[t][1]), qk(buf[t][2]), qk(buf[t][3]), t); FENCE(); };
  auto refill = [&](int d) {
    pace();
#pragma unroll
    for (int i = 0; i < 4; ++i) { buf[d][i] = ld16<AUX_NT>(rk, voff + i * 1024, (NPRE + d) * PSTR); FENCE(); }
  };
  eatk(0); refill(0); eatk(1);
#pragma unroll
  for (int t = 2; t < NPRE / 2 + 1; ++t) eatk(t);
  refill(1);
#pragma unroll
  for (int t = NPRE / 2 + 1; t < NPRE; ++t) eatk(t);
#pragma nounroll
  for (int t = NPRE; t < 32; t += 2) {
    const rsrc_t rn = t < 30 ? rk : rv;
#pragma unroll
    for (int d = 0; d < 2; ++d) {
      pace();
      float dd[4];
      const u32 so = ((t + d + 2) & 31) * PSTR;
#pragma unroll
      for (int i = 0; i < 4; ++i) { dd[i] = qk(buf[d][i]); asm volatile("" : "+v"(dd[i])); buf[d][i] = ld16<AUX_NT>(rn, voff + i * 1024, so); FENCE(); }
      reduce(dd[0], dd[1], dd[2], dd[3], t + d); FENCE();
    }
  }
  if (w == 0) STAMP(3);
  {
    constexpr float C2 = 0.0625f * 1.4426950408889634f;
    const v4f sv = *(LAS v4f*)(sc + lane * 16);
    const float m = wave_max(fmaxf(fmaxf(sv.x, sv.y), fmaxf(sv.z, sv.w))) * C2;
    const float p0 = __builtin_amdgcn_exp2f(fmaf(sv.x, C2, -m)), p1 = __builtin_amdgcn_exp2f(fmaf(sv.y, C2, -m));
    const float p2 = __builtin_amdgcn_exp2f(fmaf(sv.z, C2, -m)), p3 = __builtin_amdgcn_exp2f(fmaf(sv.w, C2, -m));
    *(LAS v4u*)(sc + lane * 16) = v4u{pk(p0, p0), pk(p1, p1), pk(p2, p2), pk(p3, p3)};
    const float l = wave_sum((p0 + p1) + (p2 + p3));
    st8<AUX_SC1>(v2u{safe(__float_as_uint(m)), safe(__float_as_uint(l))}, mkrs(a.ws, WS_END), lane == 0 ? WS_ML + bh * 256 + (s * 4 + w) * 8 : OOB, 0);
  }
  v4u msk;
  {
    const int g = lane >> 4, i = lane & 15;
    const u32 mm = ((i >> 3) == (g & 1)) ? ((i & 1) ? 0xffff0000u : 0x0000ffffu) : 0u;
    const int t = (i & 7) >> 1;
    msk = v4u{t == 0 ? mm : 0u, t == 1 ? mm : 0u, t == 2 ? mm : 0u, t == 3 ? mm : 0u};
  }
  v4f acc = {0.f, 0.f, 0.f, 0.f};
  LAS char* pr = sc + (lane >> 5) * 16;
#pragma nounroll
  for (int t = 0; t < 32; t += 2) {
    const u32 vo = t < 30 ? voff : OOB;
#pragma unroll
    for (int d = 0; d < 2; ++d) {
      const v4u p = lr16(pr + (t + d) * 32);
      if (t < 30) pace();
      const u32 so = (t + d + 2) * PSTR;
#pragma unroll
      for (int i = 0; i < 4; ++i) {
        acc = mfma(msk & p[i], buf[d][i], acc); asm volatile("" : "+v"(acc));
        buf[d][i] = ld16<AUX_NT>(rv, vo + i * 1024, so); FENCE();
      }
    }
  }
  *(LAS v4f*)(L + L_ACC + w * 1024 + ((lane >> 5) * 16 + (lane & 15)) * 32 + ((lane >> 4) & 1) * 16) = acc;
  if (w == 0) STAMP(4);
  bar();
  STAMP_DUMP();
}

DEV void helper_wave(const Args& a, LAS char* L, int hw, int lane, u32 t0, u32 pf) {
  STAMP_DECL;
  const int blk = blockIdx.x, bh = blk >> 3, s = blk & 7, g = lane >> 4, n = lane & 15;
  const rsrc_t rws = mkrs(a.ws, WS_END);
  const bool wo_role = bh < 28, fin_role = bh < 7;
  const int rho = 31 - bh, fpair = (rho >> 1) * 8 + s, fj = 2 * fpair + (rho & 1);
  const bool ffn_role = rho < 8 && fpair < 28;
  const v2u ep = ld8<AUX_SC1>(rws, WS_EPOCH, 0);
  auto get_tag = [&]() { return (((u64)(u32)__builtin_amdgcn_readfirstlane(ep.y) << 32) | (u32)__builtin_amdgcn_readfirstlane(ep.x)) + 1; };

  if (hw == 0) {
    const rsrc_t rx = mkrs(a.x, 2 * D * 2), rq = mkrs(a.wq, D * HD * 2);
    const int rg = s >> 1, cgp = (s & 1) * 32 + bh;
    const v4u x0 = ld16<AUX_NT>(rx, lane < 56 ? lane * 16 : OOB, 0), x1 = ld16<AUX_NT>(rx, lane < 56 ? lane * 16 : OOB, D * 2);
    FENCE();
#pragma unroll
    for (int i = 0; i < 14; ++i) dma16(rq, L + L_WQ + i * 1024, (lane >> 3) * (HD * 2) + (lane & 7) * 16 + cgp * 128, (rg * 112 + i * 8) * HD * 2);
    FENCE();
    auto ln = [&](v4u x, int b) {
      float v[8] = {lo16(x.x), hi16(x.x), lo16(x.y), hi16(x.y), lo16(x.z), hi16(x.z), lo16(x.w), hi16(x.w)};
      const float mean = wave_sum(((v[0] + v[1]) + (v[2] + v[3])) + ((v[4] + v[5]) + (v[6] + v[7]))) * (1.f / D);
      float ss = 0.f;
#pragma unroll
      for (int j = 0; j < 8; ++j) { v[j] -= mean; ss = fmaf(v[j], v[j], ss); }
      const float r = __builtin_amdgcn_rsqf(wave_sum(lane < 56 ? ss : 0.f) * (1.f / D) + 1e-5f);
      if (lane < 56) *(LAS v4u*)(L + L_XN + b * (D * 2) + lane * 16) = v4u{pk(v[0] * r, v[1] * r), pk(v[2] * r, v[3] * r), pk(v[4] * r, v[5] * r), pk(v[6] * r, v[7] * r)};
    };
    ln(x0, 0); ln(x1, 1);
    asm volatile("s_waitcnt vmcnt(0) lgkmcnt(0)" ::: "memory");
    STAMP(8);
    v4f qa[4];
#pragma unroll
    for (int c = 0; c < 4; ++c) qa[c] = v4f{0.f, 0.f, 0.f, 0.f};
#pragma unroll
    for (int i = 0; i < 4; ++i) {
      const int gg = i < 3 ? g : (g & 1);
      v4u xf = lr16(L + L_XN + (n & 1) * (D * 2) + (rg * 112 + i * 32 + gg * 8) * 2);
      if (i == 3 && g >= 2) xf = v4u{0u, 0u, 0u, 0u};
#pragma unroll
      for (int c = 0; c < 4; ++c) {
        LAS char* p = L + L_WQ + (i * 32 + gg * 8 + (n >> 2)) * 128 + c * 32 + (n & 3) * 8;
        qa[c] = mfma(trfrag(p, p + 512), xf, qa[c]);
      }
    }
    const u64 tag = get_tag();
    const u32 tlo = (u32)tag & 0xffu, thi = (u32)(tag >> 8);
    const u32 qo = ((n & 1) * 16 + (cgp >> 2)) * 8192 + rg * 2048 + ((cgp & 3) * 64 + (n >> 3) * 16 + g * 4 + ((n >> 1) & 3)) * 8;
#pragma unroll
    for (int c = 0; c < 2; ++c) {
      v4f ta = qa[2 * c], tb = qa[2 * c + 1];
      asm volatile("" : "+v"(ta), "+v"(tb));
      const float t0_ = sel(n & 8, tb.x, ta.x), t1_ = sel(n & 8, tb.y, ta.y), t2_ = sel(n & 8, tb.z, ta.z), t3_ = sel(n & 8, tb.w, ta.w);
      const float qv = sel(n & 4, sel(n & 2, t3_, t2_), sel(n & 2, t1_, t0_));
      st8<AUX_SC1>(v2u{(__float_as_uint(qv) & ~0xffu) | tlo, thi}, rws, qo + c * 256, WS_Q);
    }
    STAMP(9);
    v4u gq[8];
_Pragma("nounroll")
    for (int sp = 0; sp < SPIN_CAP; ++sp) {
      u32 bad = 0;
#pragma unroll
      for (int i = 0; i < 8; ++i) gq[i] = ld16<AUX_SC1>(rws, lane * 16 + (i & 3) * 1024, WS_Q + bh * 8192 + (i >> 2) * 4096);
#pragma unroll
      for (int i = 0; i < 8; ++i) bad |= (gq[i].y ^ thi) | (gq[i].w ^ thi) | (((gq[i].x ^ tlo) | (gq[i].z ^ tlo)) & 0xffu);
      if (__all(bad == 0)) break;
      __builtin_amdgcn_s_sleep(4); FENCE();
    }
#pragma unroll
    for (int i = 0; i < 2; ++i) {
      auto f = [&](u32 x) { return __uint_as_float(x & ~0xffu); };
      *(LAS u32*)(L + L_Q + i * 256 + lane * 4) = pk((f(gq[i].x) + f(gq[i + 2].x)) + (f(gq[i + 4].x) + f(gq[i + 6].x)),
                                                     (f(gq[i].z) + f(gq[i + 2].z)) + (f(gq[i + 4].z) + f(gq[i + 6].z)));
    }
    STAMP(10);
  } else if (hw == 1) {
    const v4u e4 = {EMPTY, EMPTY, EMPTY, EMPTY};
    st16<AUX_SC1>(e4, rws, lane * 16, WS_CLR + blk * CLR_PER_WG);
    st16<AUX_SC1>(e4, rws, lane < 32 ? 1024 + lane * 16 : OOB, WS_CLR + blk * CLR_PER_WG);
    asm volatile("s_waitcnt vmcnt(0)" ::: "memory");
    const u64 tag = get_tag();
    st8<AUX_SC1>(v2u{(u32)tag, (u32)(tag >> 32)}, rws, lane == 0 ? WS_FLAG + blk * 8 : OOB, 0);
    STAMP(11);
_Pragma("nounroll")
    for (int sp = 0; sp < SPIN_CAP; ++sp) {
      __builtin_amdgcn_s_sleep(16); FENCE();
      bool ok = true;
      v2u f[4];
#pragma unroll
      for (int i = 0; i < 4; ++i) f[i] = ld8<AUX_SC1>(rws, lane * 8 + i * 512, WS_FLAG);
#pragma unroll
      for (int i = 0; i < 4; ++i) ok &= f[i].x == (u32)tag && f[i].y == (u32)(tag >> 32);
      if (__all(ok)) break;
    }
    STAMP(12);
  } else if (hw == 2) {
    if (lane == 0) *(LAS u32*)(L + L_CNT) = 0u;
  }
  asm volatile("" :: "v"(pf));
  bar();
  if (hw == 0 ? wo_role : ffn_role) {
    const rsrc_t rw = mkrs(hw == 0 ? a.wo : hw == 1 ? a.wg : hw == 2 ? a.wu : a.wd, hw == 0 ? HD * D * 2 : D * F * 2);
    const u32 vo = hw == 0 ? (s * 512 + (lane >> 1)) * (D * 2) + (lane & 1) * 16 + bh * 32
                 : hw == 3 ? lane * 16 + fj * (32 * D * 2) : (lane >> 2) * (F * 2) + (lane & 3) * 16 + fj * 64;
    const u32 st = hw == 0 ? 32 * D * 2 : hw == 3 ? 1024 : 16 * F * 2;
    LAS char* dst = L + (hw == 0 ? L_WO : L_WG + (hw - 1) * 28672);
    const int cnt = hw == 0 ? 16 : 28;
    const u32 step = hw == 0 ? P_WSPAN / 16 : P_WSPAN / 28;
#pragma nounroll
    for (int i = 0; i < cnt; ++i) {
      while (now() - t0 < P_WBEG + (u32)i * step) __builtin_amdgcn_s_sleep(4);
      dma16(rw, dst + i * 1024, vo, i * st);
    }
  }
  asm volatile("s_waitcnt vmcnt(0)" ::: "memory");
  if (lane == 0) __hip_atomic_fetch_add((LAS u32*)(L + L_CNT), 1u, __ATOMIC_RELAXED, __HIP_MEMORY_SCOPE_WORKGROUP);
_Pragma("nounroll")
  for (int sp = 0; sp < SPIN_CAP; ++sp) {
    if (__builtin_amdgcn_readfirstlane(*(volatile LAS u32*)(L + L_CNT)) == 4u) break;
    __builtin_amdgcn_s_sleep(8);
  }
  FENCE();
  if (hw == 3) STAMP(13);

  const u64 tag = get_tag();
  v4u wof[16], guf[14], wdf[7];
  v4f cs = {0.f, 0.f, 0.f, 0.f}, xc = {0.f, 0.f, 0.f, 0.f};
  float fac[4] = {0.f, 0.f, 0.f, 0.f};
  constexpr u32 ONES = 0x3F803F80u;
#pragma unroll
  for (int t = 0; t < 16; ++t) { LAS char* p = L + L_WO + t * 1024 + g * 256 + n * 8; wof[t] = trfrag(p, p + 128); }
#pragma unroll
  for (int t = 0; t < 14; ++t) {
    LAS char* p = L + L_WG + ((n >> 1) & 1) * 28672 + (t * 32 + g * 8 + (n >> 2)) * 64 + hw * 16 + (n & 1) * 8;
    guf[t] = trfrag(p, p + 256);
    cs = mfma(guf[t], v4u{ONES, ONES, ONES, ONES}, cs);
  }
#pragma unroll
  for (int t = 0; t < 7; ++t) { LAS char* p = L + L_WD + (g * 8 + (n >> 2)) * (D * 2) + (hw * 7 + t) * 32 + (n & 3) * 8; wdf[t] = trfrag(p, p + 4 * D * 2); }
  const int hv = (hw * 32 + (g >> 1) * 16 + n) * 8 + (g & 1) * 4;
  {
    const v2u xx = ld8<AUX_NT>(mkrs(a.x, 2 * D * 2), hv * 2, 0);
    xc = v4f{lo16(xx.x), hi16(xx.x), lo16(xx.y), hi16(xx.y)};
  }
  if (hw == 1) {
    v2u ml;
_Pragma("nounroll")
    for (int sp = 0; sp < SPIN_CAP; ++sp) {
      ml = ld8<AUX_SC1>(rws, (lane & 31) * 8, WS_ML + bh * 256);
      if (__all(max(ml.x, ml.y) != EMPTY)) break;
      __builtin_amdgcn_s_sleep(16); FENCE();
    }
    const float m = __uint_as_float(ml.x), M = wave_max(m), e = __builtin_amdgcn_exp2f(m - M);
    const float f = e / (0.5f * wave_sum(e * __uint_as_float(ml.y)));
#pragma unroll
    for (int i = 0; i < 4; ++i) fac[i] = __uint_as_float(__builtin_amdgcn_readlane(__float_as_uint(f), s * 4 + i));
    STAMP(14);
  }
  v4u sel0, sel1;
  {
    const u32 one = (n & 1) ? 0x3F800000u : 0x00003F80u;
    const int t = (n & 7) >> 1;
    const v4u d = {t == 0 ? one : 0u, t == 1 ? one : 0u, t == 2 ? one : 0u, t == 3 ? one : 0u}, z = {0u, 0u, 0u, 0u};
    sel0 = n < 8 ? d : z; sel1 = n < 8 ? z : d;
  }

  int pass = P_DRY ? 0 : 1;
  asm volatile("" : "+s"(pass));
#pragma nounroll
  for (; pass < 2; ++pass) {
    const rsrc_t rt = mkrs(a.ws, pass ? WS_END : 0u);
    if (pass) bar();
    if (hw == 1) {
      if (pass) STAMP(16);
      v4f o = *(LAS v4f*)(L + L_ACC + lane * 16) * fac[0];
#pragma unroll
      for (int i = 1; i < 4; ++i) o += *(LAS v4f*)(L + L_ACC + i * 1024 + lane * 16) * fac[i];
      st8<AUX_SC1>(v2u{safe(pk(o.x, o.y)), safe(pk(o.z, o.w))}, rt, (lane >> 1) * 256 + (lane & 1) * 8, WS_O + (bh & 15) * 8192 + ((bh >> 4) * 8 + s) * 16);
      if (pass) STAMP(17);
    }
    if (hw == 0 && wo_role) {
      v4f y;
_Pragma("nounroll")
      for (int sp = 0; sp < SPIN_CAP; ++sp) {
        v4u o[16];
#pragma unroll
        for (int t = 0; t < 16; ++t) o[t] = ld16<AUX_SC1>(rt, lane * 16 + (t & 3) * 1024, WS_O + s * 16384 + (t >> 2) * 4096);
        FENCE();
        u32 mx = 0;
        y = v4f{0.f, 0.f, 0.f, 0.f};
#pragma unroll
        for (int t = 0; t < 16; ++t) { y = mfma(o[t], wof[t], y); mx = max(mx, umax4(o[t])); }
        if (__all(mx != EMPTY)) break;
        FENCE();
      }
      if (pass) STAMP(18);
      float yy = (y.x + y.y) + (y.z + y.w);
      yy = add16(yy, yy);
      const u32 d0 = safe(pk(yy, dpp<X1>(yy)));
      st16<AUX_SC1>(v4u{d0, dppu<SHL2>(d0), dppu<SHL4>(d0), dppu<SHL6>(d0)}, rt, (lane & 23) == 0 ? (lane >> 5) * (D * 2) + (lane & 8) * 2 : OOB, WS_Y + s * 1792 + bh * 32);
      if (pass) STAMP(19);
    }
    if (ffn_role) {
      v4u yl[4];
      const u32 yo0 = g * 1792 + (hw * 32 + n) * 16, yo1 = hw < 3 ? yo0 + 256 : OOB;
_Pragma("nounroll")
      for (int sp = 0; sp < SPIN_CAP; ++sp) {
        yl[0] = ld16<AUX_SC1>(rt, yo0, WS_Y); yl[1] = ld16<AUX_SC1>(rt, yo0, WS_Y + 4 * 1792);
        yl[2] = ld16<AUX_SC1>(rt, yo1, WS_Y); yl[3] = ld16<AUX_SC1>(rt, yo1, WS_Y + 4 * 1792);
        FENCE();
        if (__all(max(max(umax4(yl[0]), umax4(yl[1])), max(umax4(yl[2]), umax4(yl[3]))) != EMPTY)) break;
        FENCE();
      }
      if (pass && hw == 2) STAMP(20);
      v4f h = mfma(sel0, yl[0], xc);
      h = mfma(sel0, yl[1], h); h = mfma(sel1, yl[2], h); h = mfma(sel1, yl[3], h);
      if (hv < 2 * D) *(LAS v2u*)(L + L_HB + hv * 2) = v2u{pk(h.x, h.y), pk(h.z, h.w)};
      st16<AUX_SC1>(v4u{safe(__float_as_uint(h.x)), safe(__float_as_uint(h.y)), safe(__float_as_uint(h.z)), safe(__float_as_uint(h.w))}, rt,
                    (hv >> 4) == fj ? (hv & 15) * 4 : OOB, WS_F + fj * 4096 + 56 * 64);
      if (pass) bar();
      if (pass && hw == 2) STAMP(21);
      const int sl = lane < 56 ? lane : 0;
      const v4u h0 = lr16(L + L_HB + sl * 16), h1 = lr16(L + L_HB + D * 2 + sl * 16);
      v4f gu = {0.f, 0.f, 0.f, 0.f};
#pragma unroll
      for (int t = 0; t < 14; ++t) gu = mfma(guf[t], lr16(L + L_HB + (n & 1) * (D * 2) + t * 64 + g * 16), gu);
      float s0 = 0.f, s1 = 0.f, q0 = 0.f, q1 = 0.f;
#pragma unroll
      for (int i = 0; i < 4; ++i) { s0 = dot2(h0[i], ONES, s0); s1 = dot2(h1[i], ONES, s1); q0 = dot2(h0[i], h0[i], q0); q1 = dot2(h1[i], h1[i], q1); }
      if (lane >= 56) { s0 = 0.f; s1 = 0.f; q0 = 0.f; q1 = 0.f; }
      const float sa = s0 + dpp<X1>(s0), sb = s1 + dpp<X1>(s1), qa = q0 + dpp<X1>(q0), qb = q1 + dpp<X1>(q1);
      float sm = (n & 1) ? sb : sa, sq = (n & 1) ? qb : qa;
      sm += dpp<X2>(sm); sq += dpp<X2>(sq); sm += dpp<ROR4>(sm); sq += dpp<ROR4>(sq); sm += dpp<ROR8>(sm); sq += dpp<ROR8>(sq);
      sm = add16(sm, sm); sq = add16(sq, sq); sm = add32(sm, sm); sq = add32(sq, sq);
      const float mean = sm * (1.f / D), r2 = __builtin_amdgcn_rcpf(fmaxf(fmaf(-mean, mean, sq * (1.f / D)), 0.f) + 1e-5f);
      const v4f c = gu - mean * cs;
      const u32 gd = pk(mul32(c.x, c.z) * r2, mul32(c.y, c.w) * r2);
      if (n < 2) *(LAS u32*)(L + L_GU + n * 64 + hw * 16 + (g & 1) * 8 + (g >> 1) * 4) = gd;
      if (pass) bar();
      if (pass && hw == 2) STAMP(22);
      const v4u gf = lr16(L + L_GU + (n & 1) * 64 + g * 16);
      const u32 fo = n < 2 ? n * (28 * 4096) + g * 16 : OOB;
#pragma unroll
      for (int t = 0; t < 7; ++t) {
        const v4f o = mfma(wdf[t], gf, v4f{0.f, 0.f, 0.f, 0.f});
        st16<AUX_SC1>(v4u{safe(__float_as_uint(o.x)), safe(__float_as_uint(o.y)), safe(__float_as_uint(o.z)), safe(__float_as_uint(o.w))}, rt, fo,
                      WS_F + (hw * 7 + t) * 4096 + fj * 64);
      }
      if (pass && hw == 2) STAMP(23);
    }
    if (hw == 2 && fin_role) {
      v4u f[4];
_Pragma("nounroll")
      for (int sp = 0; sp < SPIN_CAP; ++sp) {
#pragma unroll
        for (int i = 0; i < 4; ++i) f[i] = ld16<AUX_SC1>(rt, (i < 3 || lane < 36) ? lane * 16 + i * 1024 : OOB, WS_F + blk * 4096);
        FENCE();
        if (__all(max(max(umax4(f[0]), umax4(f[1])), max(umax4(f[2]), umax4(f[3]))) != EMPTY)) break;
        FENCE();
      }
      if (pass) STAMP(24);
      const v4f t = (__builtin_bit_cast(v4f, f[0]) + __builtin_bit_cast(v4f, f[1])) + (__builtin_bit_cast(v4f, f[2]) + __builtin_bit_cast(v4f, f[3]));
      float r = add16(add32(t.x, t.y), add32(t.z, t.w));
      r += dpp<ROR4>(r); r += dpp<ROR8>(r);
      __builtin_amdgcn_raw_buffer_store_b16((unsigned short)pk(r, r), mkrs(a.out, pass ? 2 * D * 2 : 0), n < 4 ? (n * 4 + (g & 1) * 2 + (g >> 1)) * 2 : OOB, blk * 32, 0);
      st8<AUX_SC1>(v2u{(u32)tag, (u32)(tag >> 32)}, rt, (lane == 0 && blk == 0) ? WS_EPOCH : OOB, 0);
      if (pass) STAMP(25);
    }
  }
  STAMP_DUMP();
}

__global__ void __launch_bounds__(512, 2) fused_decode(Args a) {
  extern __shared__ __attribute__((aligned(16))) char smem[];
  LAS char* L = (LAS char*)smem;
  const u32 t0 = now();
  u32 pf = 0;
  asm volatile("" :: "s"(a.x), "s"(a.K), "s"(a.V), "s"(a.wq), "s"(a.ws));
  const int lane = threadIdx.x & 63, w = __builtin_amdgcn_readfirstlane(threadIdx.x >> 6);
  if (__builtin_expect(w < 4, 1)) stream_wave(a, L, w, lane, t0, pf); else helper_wave(a, L, w - 4, lane, t0, pf);
}

extern "C" void kernel_launch(void* const* d_in, const int* in_sizes, int n_in, void* d_out, int out_size, void* d_ws, size_t ws_size, hipStream_t stream) {
  Args a;
  a.x = (const char*)d_in[0]; a.K = (const char*)d_in[1]; a.V = (const char*)d_in[2]; a.wq = (const char*)d_in[3]; a.wo = (const char*)d_in[4];
  a.wg = (const char*)d_in[6]; a.wu = (const char*)d_in[7]; a.wd = (const char*)d_in[8];
  a.out = (char*)d_out; a.ws = (char*)d_ws;
  (void)hipFuncSetAttribute((const void*)fused_decode, hipFuncAttributeMaxDynamicSharedMemorySize, L_END);
  fused_decode<<<256, 512, L_END, stream>>>(a);
}
```

```cpp
#include <hip/hip_runtime.h>
#include <cstdint>

typedef unsigned u32;
typedef unsigned long long u64;
typedef u32 v4u __attribute__((ext_vector_type(4)));
typedef u32 v2u __attribute__((ext_vector_type(2)));
typedef float v4f __attribute__((ext_vector_type(4)));
typedef float v2f __attribute__((ext_vector_type(2)));
typedef short v4s __attribute__((ext_vector_type(4)));
typedef __bf16 v2b __attribute__((ext_vector_type(2)));
typedef __bf16 v8b __attribute__((ext_vector_type(8)));
typedef __amdgpu_buffer_rsrc_t rsrc_t;
#define LAS __attribute__((address_space(3)))
#define DEV __device__ __forceinline__

#define PROBE 0
#define P_NPRE 10
#define P_TAU 3700
#define P_OFF (-1800)
#define P_IL16 0
#define P_WBEG 600
#define P_WSPAN 2240
#define P_DRY 1
#define P_CODEPF 0

constexpr int D = 448, F = 1792, HD = 4096;
constexpr int NPRE = P_NPRE;
constexpr int AUX_NT = 2, AUX_SC1 = 16;
constexpr u32 OOB = 0x80000000u, EMPTY = 0xFFFFFFFFu;
constexpr int SPIN_CAP = 1 << 16;

constexpr u32 WS_EPOCH = 0, WS_FLAG = 4096, WS_Q = 8192, WS_CLR = WS_Q + 262144;
constexpr u32 WS_ML = WS_CLR, WS_O = WS_ML + 8192, WS_Y = WS_O + 131072, WS_F = WS_Y + 16384;
constexpr u32 CLR_PER_WG = 1536, WS_END = WS_CLR + 256 * CLR_PER_WG;
static_assert(WS_F + 56 * 4096 <= WS_END, "cleared region covers every inbox");

constexpr int L_WQ = 0, L_WO = L_WQ + 16384, L_WG = L_WO + 16384, L_WU = L_WG + 28672, L_WD = L_WU + 28672;
constexpr int L_XN = L_WD + 28672, L_HB = L_XN + 1792, L_Q = L_HB + 1792, L_GU = L_Q + 512, L_SC = L_GU + 128, L_ACC = L_SC + 4096;
constexpr int L_CNT = L_ACC + 4096, L_END = L_CNT + 16;

DEV rsrc_t mkrs(const void* p, u32 n) { return __builtin_amdgcn_make_buffer_rsrc((void*)p, 0, n, 0x00020000); }
template <int AUX> DEV v4u ld16(rsrc_t r, u32 v, u32 s) { return __builtin_amdgcn_raw_buffer_load_b128(r, v, s, AUX); }
template <int AUX> DEV v2u ld8(rsrc_t r, u32 v, u32 s) { return __builtin_amdgcn_raw_buffer_load_b64(r, v, s, AUX); }
template <int AUX> DEV void st16(v4u x, rsrc_t r, u32 v, u32 s) { __builtin_amdgcn_raw_buffer_store_b128(x, r, v, s, AUX); }
template <int AUX> DEV void st8(v2u x, rsrc_t r, u32 v, u32 s) { __builtin_amdgcn_raw_buffer_store_b64(x, r, v, s, AUX); }
DEV void dma16(rsrc_t r, LAS char* dst, u32 v, u32 s) { __builtin_amdgcn_raw_ptr_buffer_load_lds(r, (LAS void*)dst, 16, v, s, 0, AUX_NT); }
DEV u32 now() { return (u32)__builtin_amdgcn_s_memrealtime(); }
DEV float dot2(u32 a, u32 b, float c) { return __builtin_amdgcn_fdot2_f32_bf16(__builtin_bit_cast(v2b, a), __builtin_bit_cast(v2b, b), c, false); }
DEV u32 pk(float lo, float hi) { return __builtin_bit_cast(u32, __builtin_convertvector((v2f{lo, hi}), v2b)); }
DEV float lo16(u32 x) { return __uint_as_float(x << 16); }
DEV float hi16(u32 x) { return __uint_as_float(x & 0xffff0000u); }
DEV v4f mfma(v4u a, v4u b, v4f c) { return __builtin_amdgcn_mfma_f32_16x16x32_bf16(__builtin_bit_cast(v8b, a), __builtin_bit_cast(v8b, b), c, 0, 0, 0); }
template <int C> DEV float dpp(float x) { return __builtin_bit_cast(float, __builtin_amdgcn_mov_dpp(__builtin_bit_cast(int, x), C, 0xf, 0xf, true)); }
template <int C> DEV u32 dppu(u32 x) { return (u32)__builtin_amdgcn_mov_dpp((int)x, C, 0xf, 0xf, true); }
constexpr int X1 = 0xB1, X2 = 0x4E, X7 = 0x141, X15 = 0x140, ROR4 = 0x124, ROR8 = 0x128, SHL2 = 0x102, SHL4 = 0x104, SHL6 = 0x106;
DEV float add16(float a, float b) { auto s = __builtin_amdgcn_permlane16_swap(__float_as_uint(a), __float_as_uint(b), false, false); return __uint_as_float(s[0]) + __uint_as_float(s[1]); }
DEV float add32(float a, float b) { auto s = __builtin_amdgcn_permlane32_swap(__float_as_uint(a), __float_as_uint(b), false, false); return __uint_as_float(s[0]) + __uint_as_float(s[1]); }
DEV float mul32(float a, float b) { auto s = __builtin_amdgcn_permlane32_swap(__float_as_uint(a), __float_as_uint(b), false, false); return __uint_as_float(s[0]) * __uint_as_float(s[1]); }
DEV float max16(float a) { auto s = __builtin_amdgcn_permlane16_swap(__float_as_uint(a), __float_as_uint(a), false, false); return fmaxf(__uint_as_float(s[0]), __uint_as_float(s[1])); }
DEV float max32(float a) { auto s = __builtin_amdgcn_permlane32_swap(__float_as_uint(a), __float_as_uint(a), false, false); return fmaxf(__uint_as_float(s[0]), __uint_as_float(s[1])); }
DEV float row_sum(float x) { x += dpp<X1>(x); x += dpp<X2>(x); x += dpp<X7>(x); x += dpp<X15>(x); return x; }
DEV float wave_sum(float x) { x = row_sum(x); x = add16(x, x); return add32(x, x); }
DEV float wave_max(float x) { x = fmaxf(x, dpp<X1>(x)); x = fmaxf(x, dpp<X2>(x)); x = fmaxf(x, dpp<X7>(x)); x = fmaxf(x, dpp<X15>(x)); return max32(max16(x)); }
DEV float sel(bool c, float a, float b) { return c ? a : b; }
DEV u32 safe(u32 x) { return min(x, EMPTY - 1u); }
DEV u32 umax4(v4u v) { return max(max(v.x, v.y), max(v.z, v.w)); }
DEV v4u lr16(LAS char* p) { return *(LAS v4u*)p; }
DEV v4u trfrag(LAS char* p0, LAS char* p1) {
  const v4s a = __builtin_amdgcn_ds_read_tr16_b64_v4i16((LAS v4s*)p0), b = __builtin_amdgcn_ds_read_tr16_b64_v4i16((LAS v4s*)p1);
  const v2u x = __builtin_bit_cast(v2u, a), y = __builtin_bit_cast(v2u, b);
  return v4u{x.x, x.y, y.x, y.y};
}
DEV void bar() { asm volatile("s_waitcnt lgkmcnt(0)\n\ts_barrier" ::: "memory"); }
#define FENCE() do { asm volatile("" ::: "memory"); __builtin_amdgcn_sched_barrier(0); } while (0)

#define STAMP(i) do { if ((i) == PROBE) stv = now() - t0; } while (0)
#define STAMP_DECL u32 stv = 0
#define STAMP_DUMP() do { while (now() - t0 < 10000u + 10u * stv) __builtin_amdgcn_s_sleep(1); } while (0)

struct Args { const char *x, *K, *V, *wq, *wo, *wg, *wu, *wd; char *out, *ws; };

DEV void stream_wave(const Args& a, LAS char* L, int w, int lane, u32 t0, u32 pf) {
  STAMP_DECL;
  const int blk = blockIdx.x, bh = blk >> 3, s = blk & 7;
  const rsrc_t rk = mkrs(a.K + ((size_t)bh << 22), 1u << 22), rv = mkrs(a.V + ((size_t)bh << 22), 1u << 22);
  constexpr u32 PSTR = P_IL16 ? 131072 : 16384, SSTR = P_IL16 ? 16384 : 524288;
  const u32 voff = lane * 16 + w * 4096 + s * SSTR;
  int lim = P_OFF;
  auto pace = [&]() { while ((int)((now() - t0) << 6) < lim) __builtin_amdgcn_s_sleep(1); lim += P_TAU; };
  v4u buf[NPRE][4];
#pragma unroll
  for (int t = 0; t < NPRE; ++t) {
    if (t) pace(); else lim += P_TAU;
#pragma unroll
    for (int i = 0; i < 4; ++i) {
      if (t >= 2) asm volatile("s_waitcnt vmcnt(7)" ::: "memory");
      if (t == 2 && i == 0 && w == 0) STAMP(5);
      buf[t][i] = ld16<AUX_NT>(rk, voff + i * 1024, t * PSTR); FENCE();
    }
  }
  asm volatile("" :: "v"(pf));
  if (w == 0) STAMP(1);
  bar();
  if (w == 0) STAMP(2);
  const v4u q = lr16(L + L_Q + (lane & 31) * 16);
  LAS char* sc = L + L_SC + w * 1024;
  LAS char* scw = sc + (lane >> 5) * 16 + (((lane >> 4) & 1) + ((lane >> 2) & 2)) * 4;
  const bool hi8 = lane & 8, writer = (lane & 7) == 0;
  auto qk = [&](v4u k) { return dot2(k.w, q.w, dot2(k.z, q.z, dot2(k.y, q.y, dot2(k.x, q.x, 0.f)))); };
  auto reduce = [&](float d0, float d1, float d2, float d3, int t) {
    const float e0 = add16(d0, d1), e1 = add16(d2, d3);
    const float f0 = e0 + dpp<ROR8>(e0), f1 = e1 + dpp<ROR8>(e1);
    float f = hi8 ? f1 : f0;
    f += dpp<X7>(f); f += dpp<X2>(f); f += dpp<X1>(f);
    if (writer) *(LAS float*)(scw + t * 32) = f;
  };
  auto eatk = [&](int t) { reduce(qk(buf[t][0]), qk(buf[t][1]), qk(buf[t][2]), qk(buf[t][3]), t); FENCE(); };
  auto refill = [&](int d) {
    pace();
#pragma unroll
    for (int i = 0; i < 4; ++i) { buf[d][i] = ld16<AUX_NT>(rk, voff + i * 1024, (NPRE + d) * PSTR); FENCE(); }
  };
  eatk(0); refill(0); eatk(1);
#pragma unroll
  for (int t = 2; t < NPRE / 2 + 1; ++t) eatk(t);
  refill(1);
#pragma unroll
  for (int t = NPRE / 2 + 1; t < NPRE; ++t) eatk(t);
#pragma nounroll
  for (int t = NPRE; t < 32; t += 2) {
    const rsrc_t rn = t < 30 ? rk : rv;
#pragma unroll
    for (int d = 0; d < 2; ++d) {
      pace();
      float dd[4];
      const u32 so = ((t + d + 2) & 31) * PSTR;
#pragma unroll
      for (int i = 0; i < 4; ++i) { dd[i] = qk(buf[d][i]); asm volatile("" : "+v"(dd[i])); buf[d][i] = ld16<AUX_NT>(rn, voff + i * 1024, so); FENCE(); }
      reduce(dd[0], dd[1], dd[2], dd[3], t + d); FENCE();
    }
  }
  if (w == 0) STAMP(3);
  {
    constexpr float C2 = 0.0625f * 1.4426950408889634f;
    const v4f sv = *(LAS v4f*)(sc + lane * 16);
    const float m = wave_max(fmaxf(fmaxf(sv.x, sv.y), fmaxf(sv.z, sv.w))) * C2;
    const float p0 = __builtin_amdgcn_exp2f(fmaf(sv.x, C2, -m)), p1 = __builtin_amdgcn_exp2f(fmaf(sv.y, C2, -m));
    const float p2 = __builtin_amdgcn_exp2f(fmaf(sv.z, C2, -m)), p3 = __builtin_amdgcn_exp2f(fmaf(sv.w, C2, -m));
    *(LAS v4u*)(sc + lane * 16) = v4u{pk(p0, p0), pk(p1, p1), pk(p2, p2), pk(p3, p3)};
    const float l = wave_sum((p0 + p1) + (p2 + p3));
    st8<AUX_SC1>(v2u{safe(__float_as_uint(m)), safe(__float_as_uint(l))}, mkrs(a.ws, WS_END), lane == 0 ? WS_ML + bh * 256 + (s * 4 + w) * 8 : OOB, 0);
  }
  v4u msk;
  {
    const int g = lane >> 4, i = lane & 15;
    const u32 mm = ((i >> 3) == (g & 1)) ? ((i & 1) ? 0xffff0000u : 0x0000ffffu) : 0u;
    const int t = (i & 7) >> 1;
    msk = v4u{t == 0 ? mm : 0u, t == 1 ? mm : 0u, t == 2 ? mm : 0u, t == 3 ? mm : 0u};
  }
  v4f acc = {0.f, 0.f, 0.f, 0.f};
  LAS char* pr = sc + (lane >> 5) * 16;
#pragma nounroll
  for (int t = 0; t < 32; t += 2) {
    const u32 vo = t < 30 ? voff : OOB;
#pragma unroll
    for (int d = 0; d < 2; ++d) {
      const v4u p = lr16(pr + (t + d) * 32);
      if (t < 30) pace();
      const u32 so = (t + d + 2) * PSTR;
#pragma unroll
      for (int i = 0; i < 4; ++i) {
        acc = mfma(msk & p[i], buf[d][i], acc); asm volatile("" : "+v"(acc));
        buf[d][i] = ld16<AUX_NT>(rv, vo + i * 1024, so); FENCE();
      }
    }
  }
  *(LAS v4f*)(L + L_ACC + w * 1024 + ((lane >> 5) * 16 + (lane & 15)) * 32 + ((lane >> 4) & 1) * 16) = acc;
  if (w == 0) STAMP(4);
  if (w == 0 && lane == 0) *(LAS u32*)(L + L_CNT + 4) = stv;
  bar();
}

DEV void helper_wave(const Args& a, LAS char* L, int hw, int lane, u32 t0, u32 pf) {
  STAMP_DECL;
  const int blk = blockIdx.x, bh = blk >> 3, s = blk & 7, g = lane >> 4, n = lane & 15;
  const rsrc_t rws = mkrs(a.ws, WS_END);
  const bool wo_role = bh < 28, fin_role = bh < 7;
  const int rho = 31 - bh, fpair = (rho >> 1) * 8 + s, fj = 2 * fpair + (rho & 1);
  const bool ffn_role = rho < 8 && fpair < 28;
  const v2u ep = ld8<AUX_SC1>(rws, WS_EPOCH, 0);
  auto get_tag = [&]() { return (((u64)(u32)__builtin_amdgcn_readfirstlane(ep.y) << 32) | (u32)__builtin_amdgcn_readfirstlane(ep.x)) + 1; };

  if (hw == 0) {
    const rsrc_t rx = mkrs(a.x, 2 * D * 2), rq = mkrs(a.wq, D * HD * 2);
    const int rg = s >> 1, cgp = (s & 1) * 32 + bh;
    const v4u x0 = ld16<AUX_NT>(rx, lane < 56 ? lane * 16 : OOB, 0), x1 = ld16<AUX_NT>(rx, lane < 56 ? lane * 16 : OOB, D * 2);
    FENCE();
#pragma unroll
    for (int i = 0; i < 14; ++i) dma16(rq, L + L_WQ + i * 1024, (lane >> 3) * (HD * 2) + (lane & 7) * 16 + cgp * 128, (rg * 112 + i * 8) * HD * 2);
    FENCE();
    auto ln = [&](v4u x, int b) {
      float v[8] = {lo16(x.x), hi16(x.x), lo16(x.y), hi16(x.y), lo16(x.z), hi16(x.z), lo16(x.w), hi16(x.w)};
      const float mean = wave_sum(((v[0] + v[1]) + (v[2] + v[3])) + ((v[4] + v[5]) + (v[6] + v[7]))) * (1.f / D);
      float ss = 0.f;
#pragma unroll
      for (int j = 0; j < 8; ++j) { v[j] -= mean; ss = fmaf(v[j], v[j], ss); }
      const float r = __builtin_amdgcn_rsqf(wave_sum(lane < 56 ? ss : 0.f) * (1.f / D) + 1e-5f);
      if (lane < 56) *(LAS v4u*)(L + L_XN + b * (D * 2) + lane * 16) = v4u{pk(v[0] * r, v[1] * r), pk(v[2] * r, v[3] * r), pk(v[4] * r, v[5] * r), pk(v[6] * r, v[7] * r)};
    };
    ln(x0, 0); ln(x1, 1);
    asm volatile("s_waitcnt vmcnt(0) lgkmcnt(0)" ::: "memory");
    STAMP(8);
    v4f qa[4];
#pragma unroll
    for (int c = 0; c < 4; ++c) qa[c] = v4f{0.f, 0.f, 0.f, 0.f};
#pragma unroll
    for (int i = 0; i < 4; ++i) {
      const int gg = i < 3 ? g : (g & 1);
      v4u xf = lr16(L + L_XN + (n & 1) * (D * 2) + (rg * 112 + i * 32 + gg * 8) * 2);
      if (i == 3 && g >= 2) xf = v4u{0u, 0u, 0u, 0u};
#pragma unroll
      for (int c = 0; c < 4; ++c) {
        LAS char* p = L + L_WQ + (i * 32 + gg * 8 + (n >> 2)) * 128 + c * 32 + (n & 3) * 8;
        qa[c] = mfma(trfrag(p, p + 512), xf, qa[c]);
      }
    }
    const u64 tag = get_tag();
    const u32 tlo = (u32)tag & 0xffu, thi = (u32)(tag >> 8);
    const u32 qo = ((n & 1) * 16 + (cgp >> 2)) * 8192 + rg * 2048 + ((cgp & 3) * 64 + (n >> 3) * 16 + g * 4 + ((n >> 1) & 3)) * 8;
#pragma unroll
    for (int c = 0; c < 2; ++c) {
      v4f ta = qa[2 * c], tb = qa[2 * c + 1];
      asm volatile("" : "+v"(ta), "+v"(tb));
      const float t0_ = sel(n & 8, tb.x, ta.x), t1_ = sel(n & 8, tb.y, ta.y), t2_ = sel(n & 8, tb.z, ta.z), t3_ = sel(n & 8, tb.w, ta.w);
      const float qv = sel(n & 4, sel(n & 2, t3_, t2_), sel(n & 2, t1_, t0_));
      st8<AUX_SC1>(v2u{(__float_as_uint(qv) & ~0xffu) | tlo, thi}, rws, qo + c * 256, WS_Q);
    }
    STAMP(9);
    v4u gq[8];
_Pragma("nounroll")
    for (int sp = 0; sp < SPIN_CAP; ++sp) {
      u32 bad = 0;
#pragma unroll
      for (int i = 0; i < 8; ++i) gq[i] = ld16<AUX_SC1>(rws, lane * 16 + (i & 3) * 1024, WS_Q + bh * 8192 + (i >> 2) * 4096);
#pragma unroll
      for (int i = 0; i < 8; ++i) bad |= (gq[i].y ^ thi) | (gq[i].w ^ thi) | (((gq[i].x ^ tlo) | (gq[i].z ^ tlo)) & 0xffu);
      if (__all(bad == 0)) break;
      __builtin_amdgcn_s_sleep(4); FENCE();
    }
#pragma unroll
    for (int i = 0; i < 2; ++i) {
      auto f = [&](u32 x) { return __uint_as_float(x & ~0xffu); };
      *(LAS u32*)(L + L_Q + i * 256 + lane * 4) = pk((f(gq[i].x) + f(gq[i + 2].x)) + (f(gq[i + 4].x) + f(gq[i + 6].x)),
                                                     (f(gq[i].z) + f(gq[i + 2].z)) + (f(gq[i + 4].z) + f(gq[i + 6].z)));
    }
    STAMP(10);
  } else if (hw == 1) {
    const v4u e4 = {EMPTY, EMPTY, EMPTY, EMPTY};
    st16<AUX_SC1>(e4, rws, lane * 16, WS_CLR + blk * CLR_PER_WG);
    st16<AUX_SC1>(e4, rws, lane < 32 ? 1024 + lane * 16 : OOB, WS_CLR + blk * CLR_PER_WG);
    asm volatile("s_waitcnt vmcnt(0)" ::: "memory");
    const u64 tag = get_tag();
    st8<AUX_SC1>(v2u{(u32)tag, (u32)(tag >> 32)}, rws, lane == 0 ? WS_FLAG + blk * 8 : OOB, 0);
    STAMP(11);
_Pragma("nounroll")
    for (int sp = 0; sp < SPIN_CAP; ++sp) {
      __builtin_amdgcn_s_sleep(16); FENCE();
      bool ok = true;
      v2u f[4];
#pragma unroll
      for (int i = 0; i < 4; ++i) f[i] = ld8<AUX_SC1>(rws, lane * 8 + i * 512, WS_FLAG);
#pragma unroll
      for (int i = 0; i < 4; ++i) ok &= f[i].x == (u32)tag && f[i].y == (u32)(tag >> 32);
      if (__all(ok)) break;
    }
    STAMP(12);
  } else if (hw == 2) {
    if (lane == 0) *(LAS u32*)(L + L_CNT) = 0u;
  }
  asm volatile("" :: "v"(pf));
  bar();
  if (hw == 0 ? wo_role : ffn_role) {
    const rsrc_t rw = mkrs(hw == 0 ? a.wo : hw == 1 ? a.wg : hw == 2 ? a.wu : a.wd, hw == 0 ? HD * D * 2 : D * F * 2);
    const u32 vo = hw == 0 ? (s * 512 + (lane >> 1)) * (D * 2) + (lane & 1) * 16 + bh * 32
                 : hw == 3 ? lane * 16 + fj * (32 * D * 2) : (lane >> 2) * (F * 2) + (lane & 3) * 16 + fj * 64;
    const u32 st = hw == 0 ? 32 * D * 2 : hw == 3 ? 1024 : 16 * F * 2;
    LAS char* dst = L + (hw == 0 ? L_WO : L_WG + (hw - 1) * 28672);
    const int cnt = hw == 0 ? 16 : 28;
    const u32 step = hw == 0 ? P_WSPAN / 16 : P_WSPAN / 28;
#pragma nounroll
    for (int i = 0; i < cnt; ++i) {
      while (now() - t0 < P_WBEG + (u32)i * step) __builtin_amdgcn_s_sleep(4);
      dma16(rw, dst + i * 1024, vo, i * st);
    }
  }
  asm volatile("s_waitcnt vmcnt(0)" ::: "memory");
  if (lane == 0) __hip_atomic_fetch_add((LAS u32*)(L + L_CNT), 1u, __ATOMIC_RELAXED, __HIP_MEMORY_SCOPE_WORKGROUP);
_Pragma("nounroll")
  for (int sp = 0; sp < SPIN_CAP; ++sp) {
    if (__builtin_amdgcn_readfirstlane(*(volatile LAS u32*)(L + L_CNT)) == 4u) break;
    __builtin_amdgcn_s_sleep(8);
  }
  FENCE();
  if (hw == 3) STAMP(13);

  const u64 tag = get_tag();
  v4u wof[16], guf[14], wdf[7];
  v4f cs = {0.f, 0.f, 0.f, 0.f}, xc = {0.f, 0.f, 0.f, 0.f};
  float fac[4] = {0.f, 0.f, 0.f, 0.f};
  constexpr u32 ONES = 0x3F803F80u;
#pragma unroll
  for (int t = 0; t < 16; ++t) { LAS char* p = L + L_WO + t * 1024 + g * 256 + n * 8; wof[t] = trfrag(p, p + 128); }
#pragma unroll
  for (int t = 0; t < 14; ++t) {
    LAS char* p = L + L_WG + ((n >> 1) & 1) * 28672 + (t * 32 + g * 8 + (n >> 2)) * 64 + hw * 16 + (n & 1) * 8;
    guf[t] = trfrag(p, p + 256);
    cs = mfma(guf[t], v4u{ONES, ONES, ONES, ONES}, cs);
  }
#pragma unroll
  for (int t = 0; t < 7; ++t) { LAS char* p = L + L_WD + (g * 8 + (n >> 2)) * (D * 2) + (hw * 7 + t) * 32 + (n & 3) * 8; wdf[t] = trfrag(p, p + 4 * D * 2); }
  const int hv = (hw * 32 + (g >> 1) * 16 + n) * 8 + (g & 1) * 4;
  {
    const v2u xx = ld8<AUX_NT>(mkrs(a.x, 2 * D * 2), hv * 2, 0);
    xc = v4f{lo16(xx.x), hi16(xx.x), lo16(xx.y), hi16(xx.y)};
  }
  if (hw == 1) {
    v2u ml;
_Pragma("nounroll")
    for (int sp = 0; sp < SPIN_CAP; ++sp) {
      ml = ld8<AUX_SC1>(rws, (lane & 31) * 8, WS_ML + bh * 256);
      if (__all(max(ml.x, ml.y) != EMPTY)) break;
      __builtin_amdgcn_s_sleep(16); FENCE();
    }
    const float m = __uint_as_float(ml.x), M = wave_max(m), e = __builtin_amdgcn_exp2f(m - M);
    const float f = e / (0.5f * wave_sum(e * __uint_as_float(ml.y)));
#pragma unroll
    for (int i = 0; i < 4; ++i) fac[i] = __uint_as_float(__builtin_amdgcn_readlane(__float_as_uint(f), s * 4 + i));
    STAMP(14);
  }
  v4u sel0, sel1;
  {
    const u32 one = (n & 1) ? 0x3F800000u : 0x00003F80u;
    const int t = (n & 7) >> 1;
    const v4u d = {t == 0 ? one : 0u, t == 1 ? one : 0u, t == 2 ? one : 0u, t == 3 ? one : 0u}, z = {0u, 0u, 0u, 0u};
    sel0 = n < 8 ? d : z; sel1 = n < 8 ? z : d;
  }

  int pass = P_DRY ? 0 : 1;
  asm volatile("" : "+s"(pass));
#pragma nounroll
  for (; pass < 2; ++pass) {
    const rsrc_t rt = mkrs(a.ws, pass ? WS_END : 0u);
    if (pass) bar();
    if (hw == 1) {
      if (pass) STAMP(16);
      v4f o = *(LAS v4f*)(L + L_ACC + lane * 16) * fac[0];
#pragma unroll
      for (int i = 1; i < 4; ++i) o += *(LAS v4f*)(L + L_ACC + i * 1024 + lane * 16) * fac[i];
      st8<AUX_SC1>(v2u{safe(pk(o.x, o.y)), safe(pk(o.z, o.w))}, rt, (lane >> 1) * 256 + (lane & 1) * 8, WS_O + (bh & 15) * 8192 + ((bh >> 4) * 8 + s) * 16);
      if (pass) STAMP(17);
    }
    if (hw == 0 && wo_role) {
      v4f y;
_Pragma("nounroll")
      for (int sp = 0; sp < SPIN_CAP; ++sp) {
        v4u o[16];
#pragma unroll
        for (int t = 0; t < 16; ++t) o[t] = ld16<AUX_SC1>(rt, lane * 16 + (t & 3) * 1024, WS_O + s * 16384 + (t >> 2) * 4096);
        FENCE();
        u32 mx = 0;
        y = v4f{0.f, 0.f, 0.f, 0.f};
#pragma unroll
        for (int t = 0; t < 16; ++t) { y = mfma(o[t], wof[t], y); mx = max(mx, umax4(o[t])); }
        if (__all(mx != EMPTY)) break;
        FENCE();
      }
      if (pass) STAMP(18);
      float yy = (y.x + y.y) + (y.z + y.w);
      yy = add16(yy, yy);
      const u32 d0 = safe(pk(yy, dpp<X1>(yy)));
      st16<AUX_SC1>(v4u{d0, dppu<SHL2>(d0), dppu<SHL4>(d0), dppu<SHL6>(d0)}, rt, (lane & 23) == 0 ? (lane >> 5) * (D * 2) + (lane & 8) * 2 : OOB, WS_Y + s * 1792 + bh * 32);
      if (pass) STAMP(19);
    }
    if (ffn_role) {
      v4u yl[4];
      const u32 yo0 = g * 1792 + (hw * 32 + n) * 16, yo1 = hw < 3 ? yo0 + 256 : OOB;
_Pragma("nounroll")
      for (int sp = 0; sp < SPIN_CAP; ++sp) {
        yl[0] = ld16<AUX_SC1>(rt, yo0, WS_Y); yl[1] = ld16<AUX_SC1>(rt, yo0, WS_Y + 4 * 1792);
        yl[2] = ld16<AUX_SC1>(rt, yo1, WS_Y); yl[3] = ld16<AUX_SC1>(rt, yo1, WS_Y + 4 * 1792);
        FENCE();
        if (__all(max(max(umax4(yl[0]), umax4(yl[1])), max(umax4(yl[2]), umax4(yl[3]))) != EMPTY)) break;
        FENCE();
      }
      if (pass && hw == 2) STAMP(20);
      v4f h = mfma(sel0, yl[0], xc);
      h = mfma(sel0, yl[1], h); h = mfma(sel1, yl[2], h); h = mfma(sel1, yl[3], h);
      if (hv < 2 * D) *(LAS v2u*)(L + L_HB + hv * 2) = v2u{pk(h.x, h.y), pk(h.z, h.w)};
      st16<AUX_SC1>(v4u{safe(__float_as_uint(h.x)), safe(__float_as_uint(h.y)), safe(__float_as_uint(h.z)), safe(__float_as_uint(h.w))}, rt,
                    (hv >> 4) == fj ? (hv & 15) * 4 : OOB, WS_F + fj * 4096 + 56 * 64);
      if (pass) bar();
      if (pass && hw == 2) STAMP(21);
      const int sl = lane < 56 ? lane : 0;
      const v4u h0 = lr16(L + L_HB + sl * 16), h1 = lr16(L + L_HB + D * 2 + sl * 16);
      v4f gu = {0.f, 0.f, 0.f, 0.f};
#pragma unroll
      for (int t = 0; t < 14; ++t) gu = mfma(guf[t], lr16(L + L_HB + (n & 1) * (D * 2) + t * 64 + g * 16), gu);
      float s0 = 0.f, s1 = 0.f, q0 = 0.f, q1 = 0.f;
#pragma unroll
      for (int i = 0; i < 4; ++i) { s0 = dot2(h0[i], ONES, s0); s1 = dot2(h1[i], ONES, s1); q0 = dot2(h0[i], h0[i], q0); q1 = dot2(h1[i], h1[i], q1); }
      if (lane >= 56) { s0 = 0.f; s1 = 0.f; q0 = 0.f; q1 = 0.f; }
      const float sa = s0 + dpp<X1>(s0), sb = s1 + dpp<X1>(s1), qa = q0 + dpp<X1>(q0), qb = q1 + dpp<X1>(q1);
      float sm = (n & 1) ? sb : sa, sq = (n & 1) ? qb : qa;
      sm += dpp<X2>(sm); sq += dpp<X2>(sq); sm += dpp<ROR4>(sm); sq += dpp<ROR4>(sq); sm += dpp<ROR8>(sm); sq += dpp<ROR8>(sq);
      sm = add16(sm, sm); sq = add16(sq, sq); sm = add32(sm, sm); sq = add32(sq, sq);
      const float mean = sm * (1.f / D), r2 = __builtin_amdgcn_rcpf(fmaxf(fmaf(-mean, mean, sq * (1.f / D)), 0.f) + 1e-5f);
      const v4f c = gu - mean * cs;
      const u32 gd = pk(mul32(c.x, c.z) * r2, mul32(c.y, c.w) * r2);
      if (n < 2) *(LAS u32*)(L + L_GU + n * 64 + hw * 16 + (g & 1) * 8 + (g >> 1) * 4) = gd;
      if (pass) bar();
      if (pass && hw == 2) STAMP(22);
      const v4u gf = lr16(L + L_GU + (n & 1) * 64 + g * 16);
      const u32 fo = n < 2 ? n * (28 * 4096) + g * 16 : OOB;
#pragma unroll
      for (int t = 0; t < 7; ++t) {
        const v4f o = mfma(wdf[t], gf, v4f{0.f, 0.f, 0.f, 0.f});
        st16<AUX_SC1>(v4u{safe(__float_as_uint(o.x)), safe(__float_as_uint(o.y)), safe(__float_as_uint(o.z)), safe(__float_as_uint(o.w))}, rt, fo,
                      WS_F + (hw * 7 + t) * 4096 + fj * 64);
      }
      if (pass && hw == 2) STAMP(23);
    }
    if (hw == 2 && fin_role) {
      v4u f[4];
_Pragma("nounroll")
      for (int sp = 0; sp < SPIN_CAP; ++sp) {
#pragma unroll
        for (int i = 0; i < 4; ++i) f[i] = ld16<AUX_SC1>(rt, (i < 3 || lane < 36) ? lane * 16 + i * 1024 : OOB, WS_F + blk * 4096);
        FENCE();
        if (__all(max(max(umax4(f[0]), umax4(f[1])), max(umax4(f[2]), umax4(f[3]))) != EMPTY)) break;
        FENCE();
      }
      if (pass) STAMP(24);
      const v4f t = (__builtin_bit_cast(v4f, f[0]) + __builtin_bit_cast(v4f, f[1])) + (__builtin_bit_cast(v4f, f[2]) + __builtin_bit_cast(v4f, f[3]));
      float r = add16(add32(t.x, t.y), add32(t.z, t.w));
      r += dpp<ROR4>(r); r += dpp<ROR8>(r);
      __builtin_amdgcn_raw_buffer_store_b16((unsigned short)pk(r, r), mkrs(a.out, pass ? 2 * D * 2 : 0), n < 4 ? (n * 4 + (g & 1) * 2 + (g >> 1)) * 2 : OOB, blk * 32, 0);
      st8<AUX_SC1>(v2u{(u32)tag, (u32)(tag >> 32)}, rt, (lane == 0 && blk == 0) ? WS_EPOCH : OOB, 0);
      if (pass) STAMP(25);
    }
  }
  stv += __builtin_amdgcn_readfirstlane(*(volatile LAS u32*)(L + L_CNT + 4));
  STAMP_DUMP();
}

__global__ void __launch_bounds__(512, 2) fused_decode(Args a) {
  extern __shared__ __attribute__((aligned(16))) char smem[];
  LAS char* L = (LAS char*)smem;
  const u32 t0 = now();
  u32 pf = 0;
  asm volatile("" :: "s"(a.x), "s"(a.K), "s"(a.V), "s"(a.wq), "s"(a.ws));
  const int lane = threadIdx.x & 63, w = __builtin_amdgcn_readfirstlane(threadIdx.x >> 6);
  if (__builtin_expect(w < 4, 1)) stream_wave(a, L, w, lane, t0, pf); else helper_wave(a, L, w - 4, lane, t0, pf);
}

extern "C" void kernel_launch(void* const* d_in, const int* in_sizes, int n_in, void* d_out, int out_size, void* d_ws, size_t ws_size, hipStream_t stream) {
  Args a;
  a.x = (const char*)d_in[0]; a.K = (const char*)d_in[1]; a.V = (const char*)d_in[2]; a.wq = (const char*)d_in[3]; a.wo = (const char*)d_in[4];
  a.wg = (const char*)d_in[6]; a.wu = (const char*)d_in[7]; a.wd = (const char*)d_in[8];
  a.out = (char*)d_out; a.ws = (char*)d_ws;
  (void)hipFuncSetAttribute((const void*)fused_decode, hipFuncAttributeMaxDynamicSharedMemorySize, L_END);
  fused_decode<<<256, 512, L_END, stream>>>(a);
}
```

```cpp
#include <hip/hip_runtime.h>
#include <cstdint>

typedef unsigned u32;
typedef unsigned long long u64;
typedef u32 v4u __attribute__((ext_vector_type(4)));
typedef u32 v2u __attribute__((ext_vector_type(2)));
typedef float v4f __attribute__((ext_vector_type(4)));
typedef float v2f __attribute__((ext_vector_type(2)));
typedef short v4s __attribute__((ext_vector_type(4)));
typedef __bf16 v2b __attribute__((ext_vector_type(2)));
typedef __bf16 v8b __attribute__((ext_vector_type(8)));
typedef __amdgpu_buffer_rsrc_t rsrc_t;
#define LAS __attribute__((address_space(3)))
#define DEV __device__ __forceinline__

#define P_NPRE 10
#define P_TAU 3700
#define P_OFF (-1800)
#define P_IL16 0
#define P_WBEG 600
#define P_WSPAN 2240
#define P_DRY 1
#define P_CODEPF 0

constexpr int D = 448, F = 1792, HD = 4096;
constexpr int NPRE = P_NPRE;
constexpr int AUX_NT = 2, AUX_SC1 = 16;
constexpr u32 OOB = 0x80000000u, EMPTY = 0xFFFFFFFFu;
constexpr int SPIN_CAP = 1 << 16;

constexpr u32 WS_EPOCH = 0, WS_FLAG = 4096, WS_Q = 8192, WS_CLR = WS_Q + 262144;
constexpr u32 WS_ML = WS_CLR, WS_O = WS_ML + 8192, WS_Y = WS_O + 131072, WS_F = WS_Y + 16384;
constexpr u32 CLR_PER_WG = 1536, WS_END = WS_CLR + 256 * CLR_PER_WG;
static_assert(WS_F + 56 * 4096 <= WS_END, "cleared region covers every inbox");

constexpr int L_WQ = 0, L_WO = L_WQ + 16384, L_WG = L_WO + 16384, L_WU = L_WG + 28672, L_WD = L_WU + 28672;
constexpr int L_XN = L_WD + 28672, L_HB = L_XN + 1792, L_Q = L_HB + 1792, L_GU = L_Q + 512, L_SC = L_GU + 128, L_ACC = L_SC + 4096;
constexpr int L_CNT = L_ACC + 4096, L_END = L_CNT + 16;

DEV rsrc_t mkrs(const void* p, u32 n) { return __builtin_amdgcn_make_buffer_rsrc((void*)p, 0, n, 0x00020000); }
template <int AUX> DEV v4u ld16(rsrc_t r, u32 v, u32 s) { return __builtin_amdgcn_raw_buffer_load_b128(r, v, s, AUX); }
template <int AUX> DEV v2u ld8(rsrc_t r, u32 v, u32 s) { return __builtin_amdgcn_raw_buffer_load_b64(r, v, s, AUX); }
template <int AUX> DEV void st16(v4u x, rsrc_t r, u32 v, u32 s) { __builtin_amdgcn_raw_buffer_store_b128(x, r, v, s, AUX); }
template <int AUX> DEV void st8(v2u x, rsrc_t r, u32 v, u32 s) { __builtin_amdgcn_raw_buffer_store_b64(x, r, v, s, AUX); }
DEV void dma16(rsrc_t r, LAS char* dst, u32 v, u32 s) { __builtin_amdgcn_raw_ptr_buffer_load_lds(r, (LAS void*)dst, 16, v, s, 0, AUX_NT); }
DEV u32 now() { return (u32)__builtin_amdgcn_s_memrealtime(); }
DEV float dot2(u32 a, u32 b, float c) { return __builtin_amdgcn_fdot2_f32_bf16(__builtin_bit_cast(v2b, a), __builtin_bit_cast(v2b, b), c, false); }
DEV u32 pk(float lo, float hi) { return __builtin_bit_cast(u32, __builtin_convertvector((v2f{lo, hi}), v2b)); }
DEV float lo16(u32 x) { return __uint_as_float(x << 16); }
DEV float hi16(u32 x) { return __uint_as_float(x & 0xffff0000u); }
DEV v4f mfma(v4u a, v4u b, v4f c) { return __builtin_amdgcn_mfma_f32_16x16x32_bf16(__builtin_bit_cast(v8b, a), __builtin_bit_cast(v8b, b), c, 0, 0, 0); }
template <int C> DEV float dpp(float x) { return __builtin_bit_cast(float, __builtin_amdgcn_mov_dpp(__builtin_bit_cast(int, x), C, 0xf, 0xf, true)); }
template <int C> DEV u32 dppu(u32 x) { return (u32)__builtin_amdgcn_mov_dpp((int)x, C, 0xf, 0xf, true); }
constexpr int X1 = 0xB1, X2 = 0x4E, X7 = 0x141, X15 = 0x140, ROR4 = 0x124, ROR8 = 0x128, SHL2 = 0x102, SHL4 = 0x104, SHL6 = 0x106;
DEV float add16(float a, float b) { auto s = __builtin_amdgcn_permlane16_swap(__float_as_uint(a), __float_as_uint(b), false, false); return __uint_as_float(s[0]) + __uint_as_float(s[1]); }
DEV float add32(float a, float b) { auto s = __builtin_amdgcn_permlane32_swap(__float_as_uint(a), __float_as_uint(b), false, false); return __uint_as_float(s[0]) + __uint_as_float(s[1]); }
DEV float mul32(float a, float b) { auto s = __builtin_amdgcn_permlane32_swap(__float_as_uint(a), __float_as_uint(b), false, false); return __uint_as_float(s[0]) * __uint_as_float(s[1]); }
DEV float max16(float a) { auto s = __builtin_amdgcn_permlane16_swap(__float_as_uint(a), __float_as_uint(a), false, false); return fmaxf(__uint_as_float(s[0]), __uint_as_float(s[1])); }
DEV float max32(float a) { auto s = __builtin_amdgcn_permlane32_swap(__float_as_uint(a), __float_as_uint(a), false, false); return fmaxf(__uint_as_float(s[0]), __uint_as_float(s[1])); }
DEV float row_sum(float x) { x += dpp<X1>(x); x += dpp<X2>(x); x += dpp<X7>(x); x += dpp<X15>(x); return x; }
DEV float wave_sum(float x) { x = row_sum(x); x = add16(x, x); return add32(x, x); }
DEV float wave_max(float x) { x = fmaxf(x, dpp<X1>(x)); x = fmaxf(x, dpp<X2>(x)); x = fmaxf(x, dpp<X7>(x)); x = fmaxf(x, dpp<X15>(x)); return max32(max16(x)); }
DEV float sel(bool c, float a, float b) { return c ? a : b; }
DEV u32 safe(u32 x) { return min(x, EMPTY - 1u); }
DEV u32 umax4(v4u v) { return max(max(v.x, v.y), max(v.z, v.w)); }
DEV v4u lr16(LAS char* p) { return *(LAS v4u*)p; }
DEV v4u trfrag(LAS char* p0, LAS char* p1) {
  const v4s a = __builtin_amdgcn_ds_read_tr16_b64_v4i16((LAS v4s*)p0), b = __builtin_amdgcn_ds_read_tr16_b64_v4i16((LAS v4s*)p1);
  const v2u x = __builtin_bit_cast(v2u, a), y = __builtin_bit_cast(v2u, b);
  return v4u{x.x, x.y, y.x, y.y};
}
DEV void bar() { asm volatile("s_waitcnt lgkmcnt(0)\n\ts_barrier" ::: "memory"); }
#define FENCE() do { asm volatile("" ::: "memory"); __builtin_amdgcn_sched_barrier(0); } while (0)

#define STAMP(i) do {} while (0)
#define STAMP_DECL do {} while (0)
#define STAMP_DUMP() do {} while (0)

constexpr u32 WS_T0 = 32u << 20;
__global__ void k_begin(char* ws) {
  const u32 t = now();
  if (blockIdx.x == 0 && threadIdx.x == 0) *(u32*)(ws + WS_T0) = t;
}
__global__ void k_end(char* ws) {
  const u32 t = __builtin_amdgcn_readfirstlane(*(volatile u32*)(ws + WS_T0));
  for (int i = 0; i < 1000000 && now() - t < 20000u; ++i) __builtin_amdgcn_s_sleep(1);
}
struct Args { const char *x, *K, *V, *wq, *wo, *wg, *wu, *wd; char *out, *ws; };

DEV void stream_wave(const Args& a, LAS char* L, int w, int lane, u32 t0, u32 pf) {
  STAMP_DECL;
  const int blk = blockIdx.x, bh = blk >> 3, s = blk & 7;
  const rsrc_t rk = mkrs(a.K + ((size_t)bh << 22), 1u << 22), rv = mkrs(a.V + ((size_t)bh << 22), 1u << 22);
  constexpr u32 PSTR = P_IL16 ? 131072 : 16384, SSTR = P_IL16 ? 16384 : 524288;
  const u32 voff = lane * 16 + w * 4096 + s * SSTR;
  int lim = P_OFF;
  auto pace = [&]() { while ((int)((now() - t0) << 6) < lim) __builtin_amdgcn_s_sleep(1); lim += P_TAU; };
  v4u buf[NPRE][4];
#pragma unroll
  for (int t = 0; t < NPRE; ++t) {
    if (t) pace(); else lim += P_TAU;
#pragma unroll
    for (int i = 0; i < 4; ++i) {
      if (t >= 2) asm volatile("s_waitcnt vmcnt(7)" ::: "memory");
      if (t == 2 && i == 0 && w == 0) STAMP(5);
      buf[t][i] = ld16<AUX_NT>(rk, voff + i * 1024, t * PSTR); FENCE();
    }
  }
  asm volatile("" :: "v"(pf));
  if (w == 0) STAMP(1);
  bar();
  if (w == 0) STAMP(2);
  const v4u q = lr16(L + L_Q + (lane & 31) * 16);
  LAS char* sc = L + L_SC + w * 1024;
  LAS char* scw = sc + (lane >> 5) * 16 + (((lane >> 4) & 1) + ((lane >> 2) & 2)) * 4;
  const bool hi8 = lane & 8, writer = (lane & 7) == 0;
  auto qk = [&](v4u k) { return dot2(k.w, q.w, dot2(k.z, q.z, dot2(k.y, q.y, dot2(k.x, q.x, 0.f)))); };
  auto reduce = [&](float d0, float d1, float d2, float d3, int t) {
    const float e0 = add16(d0, d1), e1 = add16(d2, d3);
    const float f0 = e0 + dpp<ROR8>(e0), f1 = e1 + dpp<ROR8>(e1);
    float f = hi8 ? f1 : f0;
    f += dpp<X7>(f); f += dpp<X2>(f); f += dpp<X1>(f);
    if (writer) *(LAS float*)(scw + t * 32) = f;
  };
  auto eatk = [&](int t) { reduce(qk(buf[t][0]), qk(buf[t][1]), qk(buf[t][2]), qk(buf[t][3]), t); FENCE(); };
  auto refill = [&](int d) {
    pace();
#pragma unroll
    for (int i = 0; i < 4; ++i) { buf[d][i] = ld16<AUX_NT>(rk, voff + i * 1024, (NPRE + d) * PSTR); FENCE(); }
  };
  eatk(0); refill(0); eatk(1);
#pragma unroll
  for (int t = 2; t < NPRE / 2 + 1; ++t) eatk(t);
  refill(1);
#pragma unroll
  for (int t = NPRE / 2 + 1; t < NPRE; ++t) eatk(t);
#pragma nounroll
  for (int t = NPRE; t < 32; t += 2) {
    const rsrc_t rn = t < 30 ? rk : rv;
#pragma unroll
    for (int d = 0; d < 2; ++d) {
      pace();
      float dd[4];
      const u32 so = ((t + d + 2) & 31) * PSTR;
#pragma unroll
      for (int i = 0; i < 4; ++i) { dd[i] = qk(buf[d][i]); asm volatile("" : "+v"(dd[i])); buf[d][i] = ld16<AUX_NT>(rn, voff + i * 1024, so); FENCE(); }
      reduce(dd[0], dd[1], dd[2], dd[3], t + d); FENCE();
    }
  }
  if (w == 0) STAMP(3);
  {
    constexpr float C2 = 0.0625f * 1.4426950408889634f;
    const v4f sv = *(LAS v4f*)(sc + lane * 16);
    const float m = wave_max(fmaxf(fmaxf(sv.x, sv.y), fmaxf(sv.z, sv.w))) * C2;
    const float p0 = __builtin_amdgcn_exp2f(fmaf(sv.x, C2, -m)), p1 = __builtin_amdgcn_exp2f(fmaf(sv.y, C2, -m));
    const float p2 = __builtin_amdgcn_exp2f(fmaf(sv.z, C2, -m)), p3 = __builtin_amdgcn_exp2f(fmaf(sv.w, C2, -m));
    *(LAS v4u*)(sc + lane * 16) = v4u{pk(p0, p0), pk(p1, p1), pk(p2, p2), pk(p3, p3)};
    const float l = wave_sum((p0 + p1) + (p2 + p3));
    st8<AUX_SC1>(v2u{safe(__float_as_uint(m)), safe(__float_as_uint(l))}, mkrs(a.ws, WS_END), lane == 0 ? WS_ML + bh * 256 + (s * 4 + w) * 8 : OOB, 0);
  }
  v4u msk;
  {
    const int g = lane >> 4, i = lane & 15;
    const u32 mm = ((i >> 3) == (g & 1)) ? ((i & 1) ? 0xffff0000u : 0x0000ffffu) : 0u;
    const int t = (i & 7) >> 1;
    msk = v4u{t == 0 ? mm : 0u, t == 1 ? mm : 0u, t == 2 ? mm : 0u, t == 3 ? mm : 0u};
  }
  v4f acc = {0.f, 0.f, 0.f, 0.f};
  LAS char* pr = sc + (lane >> 5) * 16;
#pragma nounroll
  for (int t = 0; t < 32; t += 2) {
    const u32 vo = t < 30 ? voff : OOB;
#pragma unroll
    for (int d = 0; d < 2; ++d) {
      const v4u p = lr16(pr + (t + d) * 32);
      if (t < 30) pace();
      const u32 so = (t + d + 2) * PSTR;
#pragma unroll
      for (int i = 0; i < 4; ++i) {
        acc = mfma(msk & p[i], buf[d][i], acc); asm volatile("" : "+v"(acc));
        buf[d][i] = ld16<AUX_NT>(rv, vo + i * 1024, so); FENCE();
      }
    }
  }
  *(LAS v4f*)(L + L_ACC + w * 1024 + ((lane >> 5) * 16 + (lane & 15)) * 32 + ((lane >> 4) & 1) * 16) = acc;
  if (w == 0) STAMP(4);
  bar();
}

DEV void helper_wave(const Args& a, LAS char* L, int hw, int lane, u32 t0, u32 pf) {
  STAMP_DECL;
  const int blk = blockIdx.x, bh = blk >> 3, s = blk & 7, g = lane >> 4, n = lane & 15;
  const rsrc_t rws = mkrs(a.ws, WS_END);
  const bool wo_role = bh < 28, fin_role = bh < 7;
  const int rho = 31 - bh, fpair = (rho >> 1) * 8 + s, fj = 2 * fpair + (rho & 1);
  const bool ffn_role = rho < 8 && fpair < 28;
  const v2u ep = ld8<AUX_SC1>(rws, WS_EPOCH, 0);
  auto get_tag = [&]() { return (((u64)(u32)__builtin_amdgcn_readfirstlane(ep.y) << 32) | (u32)__builtin_amdgcn_readfirstlane(ep.x)) + 1; };

  if (hw == 0) {
    const rsrc_t rx = mkrs(a.x, 2 * D * 2), rq = mkrs(a.wq, D * HD * 2);
    const int rg = s >> 1, cgp = (s & 1) * 32 + bh;
    const v4u x0 = ld16<AUX_NT>(rx, lane < 56 ? lane * 16 : OOB, 0), x1 = ld16<AUX_NT>(rx, lane < 56 ? lane * 16 : OOB, D * 2);
    FENCE();
#pragma unroll
    for (int i = 0; i < 14; ++i) dma16(rq, L + L_WQ + i * 1024, (lane >> 3) * (HD * 2) + (lane & 7) * 16 + cgp * 128, (rg * 112 + i * 8) * HD * 2);
    FENCE();
    auto ln = [&](v4u x, int b) {
      float v[8] = {lo16(x.x), hi16(x.x), lo16(x.y), hi16(x.y), lo16(x.z), hi16(x.z), lo16(x.w), hi16(x.w)};
      const float mean = wave_sum(((v[0] + v[1]) + (v[2] + v[3])) + ((v[4] + v[5]) + (v[6] + v[7]))) * (1.f / D);
      float ss = 0.f;
#pragma unroll
      for (int j = 0; j < 8; ++j) { v[j] -= mean; ss = fmaf(v[j], v[j], ss); }
      const float r = __builtin_amdgcn_rsqf(wave_sum(lane < 56 ? ss : 0.f) * (1.f / D) + 1e-5f);
      if (lane < 56) *(LAS v4u*)(L + L_XN + b * (D * 2) + lane * 16) = v4u{pk(v[0] * r, v[1] * r), pk(v[2] * r, v[3] * r), pk(v[4] * r, v[5] * r), pk(v[6] * r, v[7] * r)};
    };
    ln(x0, 0); ln(x1, 1);
    asm volatile("s_waitcnt vmcnt(0) lgkmcnt(0)" ::: "memory");
    STAMP(8);
    v4f qa[4];
#pragma unroll
    for (int c = 0; c < 4; ++c) qa[c] = v4f{0.f, 0.f, 0.f, 0.f};
#pragma unroll
    for (int i = 0; i < 4; ++i) {
      const int gg = i < 3 ? g : (g & 1);
      v4u xf = lr16(L + L_XN + (n & 1) * (D * 2) + (rg * 112 + i * 32 + gg * 8) * 2);
      if (i == 3 && g >= 2) xf = v4u{0u, 0u, 0u, 0u};
#pragma unroll
      for (int c = 0; c < 4; ++c) {
        LAS char* p = L + L_WQ + (i * 32 + gg * 8 + (n >> 2)) * 128 + c * 32 + (n & 3) * 8;
        qa[c] = mfma(trfrag(p, p + 512), xf, qa[c]);
      }
    }
    const u64 tag = get_tag();
    const u32 tlo = (u32)tag & 0xffu, thi = (u32)(tag >> 8);
    const u32 qo = ((n & 1) * 16 + (cgp >> 2)) * 8192 + rg * 2048 + ((cgp & 3) * 64 + (n >> 3) * 16 + g * 4 + ((n >> 1) & 3)) * 8;
#pragma unroll
    for (int c = 0; c < 2; ++c) {
      v4f ta = qa[2 * c], tb = qa[2 * c + 1];
      asm volatile("" : "+v"(ta), "+v"(tb));
      const float t0_ = sel(n & 8, tb.x, ta.x), t1_ = sel(n & 8, tb.y, ta.y), t2_ = sel(n & 8, tb.z, ta.z), t3_ = sel(n & 8, tb.w, ta.w);
      const float qv = sel(n & 4, sel(n & 2, t3_, t2_), sel(n & 2, t1_, t0_));
      st8<AUX_SC1>(v2u{(__float_as_uint(qv) & ~0xffu) | tlo, thi}, rws, qo + c * 256, WS_Q);
    }
    STAMP(9);
    v4u gq[8];
_Pragma("nounroll")
    for (int sp = 0; sp < SPIN_CAP; ++sp) {
      u32 bad = 0;
#pragma unroll
      for (int i = 0; i < 8; ++i) gq[i] = ld16<AUX_SC1>(rws, lane * 16 + (i & 3) * 1024, WS_Q + bh * 8192 + (i >> 2) * 4096);
#pragma unroll
      for (int i = 0; i < 8; ++i) bad |= (gq[i].y ^ thi) | (gq[i].w ^ thi) | (((gq[i].x ^ tlo) | (gq[i].z ^ tlo)) & 0xffu);
      if (__all(bad == 0)) break;
      __builtin_amdgcn_s_sleep(4); FENCE();
    }
#pragma unroll
    for (int i = 0; i < 2; ++i) {
      auto f = [&](u32 x) { return __uint_as_float(x & ~0xffu); };
      *(LAS u32*)(L + L_Q + i * 256 + lane * 4) = pk((f(gq[i].x) + f(gq[i + 2].x)) + (f(gq[i + 4].x) + f(gq[i + 6].x)),
                                                     (f(gq[i].z) + f(gq[i + 2].z)) + (f(gq[i + 4].z) + f(gq[i + 6].z)));
    }
    STAMP(10);
  } else if (hw == 1) {
    const v4u e4 = {EMPTY, EMPTY, EMPTY, EMPTY};
    st16<AUX_SC1>(e4, rws, lane * 16, WS_CLR + blk * CLR_PER_WG);
    st16<AUX_SC1>(e4, rws, lane < 32 ? 1024 + lane * 16 : OOB, WS_CLR + blk * CLR_PER_WG);
    asm volatile("s_waitcnt vmcnt(0)" ::: "memory");
    const u64 tag = get_tag();
    st8<AUX_SC1>(v2u{(u32)tag, (u32)(tag >> 32)}, rws, lane == 0 ? WS_FLAG + blk * 8 : OOB, 0);
    STAMP(11);
_Pragma("nounroll")
    for (int sp = 0; sp < SPIN_CAP; ++sp) {
      __builtin_amdgcn_s_sleep(16); FENCE();
      bool ok = true;
      v2u f[4];
#pragma unroll
      for (int i = 0; i < 4; ++i) f[i] = ld8<AUX_SC1>(rws, lane * 8 + i * 512, WS_FLAG);
#pragma unroll
      for (int i = 0; i < 4; ++i) ok &= f[i].x == (u32)tag && f[i].y == (u32)(tag >> 32);
      if (__all(ok)) break;
    }
    STAMP(12);
  } else if (hw == 2) {
    if (lane == 0) *(LAS u32*)(L + L_CNT) = 0u;
  }
  asm volatile("" :: "v"(pf));
  bar();
  if (hw == 0 ? wo_role : ffn_role) {
    const rsrc_t rw = mkrs(hw == 0 ? a.wo : hw == 1 ? a.wg : hw == 2 ? a.wu : a.wd, hw == 0 ? HD * D * 2 : D * F * 2);
    const u32 vo = hw == 0 ? (s * 512 + (lane >> 1)) * (D * 2) + (lane & 1) * 16 + bh * 32
                 : hw == 3 ? lane * 16 + fj * (32 * D * 2) : (lane >> 2) * (F * 2) + (lane & 3) * 16 + fj * 64;
    const u32 st = hw == 0 ? 32 * D * 2 : hw == 3 ? 1024 : 16 * F * 2;
    LAS char* dst = L + (hw == 0 ? L_WO : L_WG + (hw - 1) * 28672);
    const int cnt = hw == 0 ? 16 : 28;
    const u32 step = hw == 0 ? P_WSPAN / 16 : P_WSPAN / 28;
#pragma nounroll
    for (int i = 0; i < cnt; ++i) {
      while (now() - t0 < P_WBEG + (u32)i * step) __builtin_amdgcn_s_sleep(4);
      dma16(rw, dst + i * 1024, vo, i * st);
    }
  }
  asm volatile("s_waitcnt vmcnt(0)" ::: "memory");
  if (lane == 0) __hip_atomic_fetch_add((LAS u32*)(L + L_CNT), 1u, __ATOMIC_RELAXED, __HIP_MEMORY_SCOPE_WORKGROUP);
_Pragma("nounroll")
  for (int sp = 0; sp < SPIN_CAP; ++sp) {
    if (__builtin_amdgcn_readfirstlane(*(volatile LAS u32*)(L + L_CNT)) == 4u) break;
    __builtin_amdgcn_s_sleep(8);
  }
  FENCE();
  if (hw == 3) STAMP(13);

  const u64 tag = get_tag();
  v4u wof[16], guf[14], wdf[7];
  v4f cs = {0.f, 0.f, 0.f, 0.f}, xc = {0.f, 0.f, 0.f, 0.f};
  float fac[4] = {0.f, 0.f, 0.f, 0.f};
  constexpr u32 ONES = 0x3F803F80u;
#pragma unroll
  for (int t = 0; t < 16; ++t) { LAS char* p = L + L_WO + t * 1024 + g * 256 + n * 8; wof[t] = trfrag(p, p + 128); }
#pragma unroll
  for (int t = 0; t < 14; ++t) {
    LAS char* p = L + L_WG + ((n >> 1) & 1) * 28672 + (t * 32 + g * 8 + (n >> 2)) * 64 + hw * 16 + (n & 1) * 8;
    guf[t] = trfrag(p, p + 256);
    cs = mfma(guf[t], v4u{ONES, ONES, ONES, ONES}, cs);
  }
#pragma unroll
  for (int t = 0; t < 7; ++t) { LAS char* p = L + L_WD + (g * 8 + (n >> 2)) * (D * 2) + (hw * 7 + t) * 32 + (n & 3) * 8; wdf[t] = trfrag(p, p + 4 * D * 2); }
  const int hv = (hw * 32 + (g >> 1) * 16 + n) * 8 + (g & 1) * 4;
  {
    const v2u xx = ld8<AUX_NT>(mkrs(a.x, 2 * D * 2), hv * 2, 0);
    xc = v4f{lo16(xx.x), hi16(xx.x), lo16(xx.y), hi16(xx.y)};
  }
  if (hw == 1) {
    v2u ml;
_Pragma("nounroll")
    for (int sp = 0; sp < SPIN_CAP; ++sp) {
      ml = ld8<AUX_SC1>(rws, (lane & 31) * 8, WS_ML + bh * 256);
      if (__all(max(ml.x, ml.y) != EMPTY)) break;
      __builtin_amdgcn_s_sleep(16); FENCE();
    }
    const float m = __uint_as_float(ml.x), M = wave_max(m), e = __builtin_amdgcn_exp2f(m - M);
    const float f = e / (0.5f * wave_sum(e * __uint_as_float(ml.y)));
#pragma unroll
    for (int i = 0; i < 4; ++i) fac[i] = __uint_as_float(__builtin_amdgcn_readlane(__float_as_uint(f), s * 4 + i));
    STAMP(14);
  }
  v4u sel0, sel1;
  {
    const u32 one = (n & 1) ? 0x3F800000u : 0x00003F80u;
    const int t = (n & 7) >> 1;
    const v4u d = {t == 0 ? one : 0u, t == 1 ? one : 0u, t == 2 ? one : 0u, t == 3 ? one : 0u}, z = {0u, 0u, 0u, 0u};
    sel0 = n < 8 ? d : z; sel1 = n < 8 ? z : d;
  }

  int pass = P_DRY ? 0 : 1;
  asm volatile("" : "+s"(pass));
#pragma nounroll
  for (; pass < 2; ++pass) {
    const rsrc_t rt = mkrs(a.ws, pass ? WS_END : 0u);
    if (pass) bar();
    if (hw == 1) {
      if (pass) STAMP(16);
      v4f o = *(LAS v4f*)(L + L_ACC + lane * 16) * fac[0];
#pragma unroll
      for (int i = 1; i < 4; ++i) o += *(LAS v4f*)(L + L_ACC + i * 1024 + lane * 16) * fac[i];
      st8<AUX_SC1>(v2u{safe(pk(o.x, o.y)), safe(pk(o.z, o.w))}, rt, (lane >> 1) * 256 + (lane & 1) * 8, WS_O + (bh & 15) * 8192 + ((bh >> 4) * 8 + s) * 16);
      if (pass) STAMP(17);
    }
    if (hw == 0 && wo_role) {
      v4f y;
_Pragma("nounroll")
      for (int sp = 0; sp < SPIN_CAP; ++sp) {
        v4u o[16];
#pragma unroll
        for (int t = 0; t < 16; ++t) o[t] = ld16<AUX_SC1>(rt, lane * 16 + (t & 3) * 1024, WS_O + s * 16384 + (t >> 2) * 4096);
        FENCE();
        u32 mx = 0;
        y = v4f{0.f, 0.f, 0.f, 0.f};
#pragma unroll
        for (int t = 0; t < 16; ++t) { y = mfma(o[t], wof[t], y); mx = max(mx, umax4(o[t])); }
        if (__all(mx != EMPTY)) break;
        FENCE();
      }
      if (pass) STAMP(18);
      float yy = (y.x + y.y) + (y.z + y.w);
      yy = add16(yy, yy);
      const u32 d0 = safe(pk(yy, dpp<X1>(yy)));
      st16<AUX_SC1>(v4u{d0, dppu<SHL2>(d0), dppu<SHL4>(d0), dppu<SHL6>(d0)}, rt, (lane & 23) == 0 ? (lane >> 5) * (D * 2) + (lane & 8) * 2 : OOB, WS_Y + s * 1792 + bh * 32);
      if (pass) STAMP(19);
    }
    if (ffn_role) {
      v4u yl[4];
      const u32 yo0 = g * 1792 + (hw * 32 + n) * 16, yo1 = hw < 3 ? yo0 + 256 : OOB;
_Pragma("nounroll")
      for (int sp = 0; sp < SPIN_CAP; ++sp) {
        yl[0] = ld16<AUX_SC1>(rt, yo0, WS_Y); yl[1] = ld16<AUX_SC1>(rt, yo0, WS_Y + 4 * 1792);
        yl[2] = ld16<AUX_SC1>(rt, yo1, WS_Y); yl[3] = ld16<AUX_SC1>(rt, yo1, WS_Y + 4 * 1792);
        FENCE();
        if (__all(max(max(umax4(yl[0]), umax4(yl[1])), max(umax4(yl[2]), umax4(yl[3]))) != EMPTY)) break;
        FENCE();
      }
      if (pass && hw == 2) STAMP(20);
      v4f h = mfma(sel0, yl[0], xc);
      h = mfma(sel0, yl[1], h); h = mfma(sel1, yl[2], h); h = mfma(sel1, yl[3], h);
      if (hv < 2 * D) *(LAS v2u*)(L + L_HB + hv * 2) = v2u{pk(h.x, h.y), pk(h.z, h.w)};
      st16<AUX_SC1>(v4u{safe(__float_as_uint(h.x)), safe(__float_as_uint(h.y)), safe(__float_as_uint(h.z)), safe(__float_as_uint(h.w))}, rt,
                    (hv >> 4) == fj ? (hv & 15) * 4 : OOB, WS_F + fj * 4096 + 56 * 64);
      if (pass) bar();
      if (pass && hw == 2) STAMP(21);
      const int sl = lane < 56 ? lane : 0;
      const v4u h0 = lr16(L + L_HB + sl * 16), h1 = lr16(L + L_HB + D * 2 + sl * 16);
      v4f gu = {0.f, 0.f, 0.f, 0.f};
#pragma unroll
      for (int t = 0; t < 14; ++t) gu = mfma(guf[t], lr16(L + L_HB + (n & 1) * (D * 2) + t * 64 + g * 16), gu);
      float s0 = 0.f, s1 = 0.f, q0 = 0.f, q1 = 0.f;
#pragma unroll
      for (int i = 0; i < 4; ++i) { s0 = dot2(h0[i], ONES, s0); s1 = dot2(h1[i], ONES, s1); q0 = dot2(h0[i], h0[i], q0); q1 = dot2(h1[i], h1[i], q1); }
      if (lane >= 56) { s0 = 0.f; s1 = 0.f; q0 = 0.f; q1 = 0.f; }
      const float sa = s0 + dpp<X1>(s0), sb = s1 + dpp<X1>(s1), qa = q0 + dpp<X1>(q0), qb = q1 + dpp<X1>(q1);
      float sm = (n & 1) ? sb : sa, sq = (n & 1) ? qb : qa;
      sm += dpp<X2>(sm); sq += dpp<X2>(sq); sm += dpp<ROR4>(sm); sq += dpp<ROR4>(sq); sm += dpp<ROR8>(sm); sq += dpp<ROR8>(sq);
      sm = add16(sm, sm); sq = add16(sq, sq); sm = add32(sm, sm); sq = add32(sq, sq);
      const float mean = sm * (1.f / D), r2 = __builtin_amdgcn_rcpf(fmaxf(fmaf(-mean, mean, sq * (1.f / D)), 0.f) + 1e-5f);
      const v4f c = gu - mean * cs;
      const u32 gd = pk(mul32(c.x, c.z) * r2, mul32(c.y, c.w) * r2);
      if (n < 2) *(LAS u32*)(L + L_GU + n * 64 + hw * 16 + (g & 1) * 8 + (g >> 1) * 4) = gd;
      if (pass) bar();
      if (pass && hw == 2) STAMP(22);
      const v4u gf = lr16(L + L_GU + (n & 1) * 64 + g * 16);
      const u32 fo = n < 2 ? n * (28 * 4096) + g * 16 : OOB;
#pragma unroll
      for (int t = 0; t < 7; ++t) {
        const v4f o = mfma(wdf[t], gf, v4f{0.f, 0.f, 0.f, 0.f});
        st16<AUX_SC1>(v4u{safe(__float_as_uint(o.x)), safe(__float_as_uint(o.y)), safe(__float_as_uint(o.z)), safe(__float_as_uint(o.w))}, rt, fo,
                      WS_F + (hw * 7 + t) * 4096 + fj * 64);
      }
      if (pass && hw == 2) STAMP(23);
    }
    if (hw == 2 && fin_role) {
      v4u f[4];
_Pragma("nounroll")
      for (int sp = 0; sp < SPIN_CAP; ++sp) {
#pragma unroll
        for (int i = 0; i < 4; ++i) f[i] = ld16<AUX_SC1>(rt, (i < 3 || lane < 36) ? lane * 16 + i * 1024 : OOB, WS_F + blk * 4096);
        FENCE();
        if (__all(max(max(umax4(f[0]), umax4(f[1])), max(umax4(f[2]), umax4(f[3]))) != EMPTY)) break;
        FENCE();
      }
      if (pass) STAMP(24);
      const v4f t = (__builtin_bit_cast(v4f, f[0]) + __builtin_bit_cast(v4f, f[1])) + (__builtin_bit_cast(v4f, f[2]) + __builtin_bit_cast(v4f, f[3]));
      float r = add16(add32(t.x, t.y), add32(t.z, t.w));
      r += dpp<ROR4>(r); r += dpp<ROR8>(r);
      __builtin_amdgcn_raw_buffer_store_b16((unsigned short)pk(r, r), mkrs(a.out, pass ? 2 * D * 2 : 0), n < 4 ? (n * 4 + (g & 1) * 2 + (g >> 1)) * 2 : OOB, blk * 32, 0);
      st8<AUX_SC1>(v2u{(u32)tag, (u32)(tag >> 32)}, rt, (lane == 0 && blk == 0) ? WS_EPOCH : OOB, 0);
      if (pass) STAMP(25);
    }
  }
  STAMP_DUMP();
}

__global__ void __launch_bounds__(512, 2) fused_decode(Args a) {
  extern __shared__ __attribute__((aligned(16))) char smem[];
  LAS char* L = (LAS char*)smem;
  const u32 t0 = now();
  u32 pf = 0;
  asm volatile("" :: "s"(a.x), "s"(a.K), "s"(a.V), "s"(a.wq), "s"(a.ws));
  const int lane = threadIdx.x & 63, w = __builtin_amdgcn_readfirstlane(threadIdx.x >> 6);
  if (__builtin_expect(w < 4, 1)) stream_wave(a, L, w, lane, t0, pf); else helper_wave(a, L, w - 4, lane, t0, pf);
}

extern "C" void kernel_launch(void* const* d_in, const int* in_sizes, int n_in, void* d_out, int out_size, void* d_ws, size_t ws_size, hipStream_t stream) {
  Args a;
  a.x = (const char*)d_in[0]; a.K = (const char*)d_in[1]; a.V = (const char*)d_in[2]; a.wq = (const char*)d_in[3]; a.wo = (const char*)d_in[4];
  a.wg = (const char*)d_in[6]; a.wu = (const char*)d_in[7]; a.wd = (const char*)d_in[8];
  a.out = (char*)d_out; a.ws = (char*)d_ws;
  (void)hipFuncSetAttribute((const void*)fused_decode, hipFuncAttributeMaxDynamicSharedMemorySize, L_END);
  k_begin<<<1, 64, 0, stream>>>(a.ws);
  fused_decode<<<256, 512, L_END, stream>>>(a);
  k_end<<<1, 64, 0, stream>>>(a.ws);
}
```

```cpp
#include <hip/hip_runtime.h>
#include <cstdint>

typedef unsigned u32;
typedef unsigned long long u64;
typedef u32 v4u __attribute__((ext_vector_type(4)));
typedef u32 v2u __attribute__((ext_vector_type(2)));
typedef float v4f __attribute__((ext_vector_type(4)));
typedef float v2f __attribute__((ext_vector_type(2)));
typedef short v4s __attribute__((ext_vector_type(4)));
typedef __bf16 v2b __attribute__((ext_vector_type(2)));
typedef __bf16 v8b __attribute__((ext_vector_type(8)));
typedef __amdgpu_buffer_rsrc_t rsrc_t;
#define LAS __attribute__((address_space(3)))
#define DEV __device__ __forceinline__

#define DEFP(n, v)
#define P_NPRE 8
#define P_NL 9
#define P_TAU 2770
#define P_OFF 4500
#define P_MAP 1
#define P_QLOCAL 1
#define P_QT 450
#define P_QDELAY 12
#define P_FLAGT 800
#define P_GLOB 0
#define P_WBEG 500
#define P_WSPAN 2900
#define P_WAUX 0
#define P_T1 95
#define P_D2 12
#define P_D3 10
#define P_YR 2
#define P_NOW 0
#define P_ABL 0
#define P_DRY 1
#define P_CODEPF 1
#define P_CODEPF_BYTES 15104

constexpr int D = 448, F = 1792, HD = 4096;
constexpr int NPRE = P_NPRE, NL = P_NL, NP = 43;
static_assert((NPRE + NL) % 2 == 1, "the steady K loop handles pairs of pieces");
constexpr int AUX_NT = 2, AUX_SC1 = 16;
constexpr u32 OOB = 0x80000000u, EMPTY = 0xFFFFFFFFu;
constexpr int SPIN_CAP = 1 << 16;
constexpr u32 LAST_ISSUE = (P_OFF + (2 * NP - 1) * P_TAU) / 64;

constexpr u32 WS_EPOCH = 0, WS_FLAG = 4096, WS_Q = 8192, WS_CLR = WS_Q + 65536;
constexpr u32 WS_ML = WS_CLR, WS_O = WS_ML + 8192, WS_Y = WS_O + 16 * 6144, YSTR = 16384, WS_F = WS_Y + 4 * YSTR, FSTR = 7424;
constexpr u32 CLR_PER_WG = 6144, WS_END = WS_CLR + 64 * CLR_PER_WG;
static_assert(WS_F + 28 * FSTR <= WS_END, "cleared region covers every inbox");

constexpr int L_WO = 0, L_WQ = 0, L_WG = 65536, L_WU = L_WG + 28672, L_WD = L_WU + 28672;
constexpr int L_XN = L_WD + 28672, L_HB = L_XN, L_HF = L_XN + 1792, L_GU = L_HF + 3584, L_ZERO = L_GU + 128, L_TB = L_ZERO + 16, L_YR = L_TB + 16, L_END = L_YR + 2048;
constexpr int L_Q = 0, L_ML = 512, L_CNT = 544, L_SC = 1024, SCW = 1408, L_ACC = L_SC + 4 * SCW, L_KB = 11264;
static_assert(L_ACC + 4096 <= L_KB && L_KB + NL * 16384 <= L_END, "streamer LDS map");
static_assert(L_END <= 163840 && L_YR % 16 == 0, "LDS budget");

DEV rsrc_t mkrs(const void* p, u32 n) { return __builtin_amdgcn_make_buffer_rsrc((void*)p, 0, n, 0x00020000); }
template <int AUX> DEV v4u ld16(rsrc_t r, u32 v, u32 s) { return __builtin_amdgcn_raw_buffer_load_b128(r, v, s, AUX); }
template <int AUX> DEV v2u ld8(rsrc_t r, u32 v, u32 s) { return __builtin_amdgcn_raw_buffer_load_b64(r, v, s, AUX); }
template <int AUX> DEV void st16(v4u x, rsrc_t r, u32 v, u32 s) { __builtin_amdgcn_raw_buffer_store_b128(x, r, v, s, AUX); }
template <int AUX> DEV void st8(v2u x, rsrc_t r, u32 v, u32 s) { __builtin_amdgcn_raw_buffer_store_b64(x, r, v, s, AUX); }
template <int AUX> DEV void dma16(rsrc_t r, LAS char* dst, u32 v, u32 s) { __builtin_amdgcn_raw_ptr_buffer_load_lds(r, (LAS void*)dst, 16, v, s, 0, AUX); }
DEV u32 now() { return (u32)__builtin_amdgcn_s_memrealtime(); }
DEV float dot2(u32 a, u32 b, float c) { return __builtin_amdgcn_fdot2_f32_bf16(__builtin_bit_cast(v2b, a), __builtin_bit_cast(v2b, b), c, false); }
DEV u32 pk(float lo, float hi) { return __builtin_bit_cast(u32, __builtin_convertvector((v2f{lo, hi}), v2b)); }
DEV float lo16(u32 x) { return __uint_as_float(x << 16); }
DEV float hi16(u32 x) { return __uint_as_float(x & 0xffff0000u); }
DEV v4f mfma(v4u a, v4u b, v4f c) { return __builtin_amdgcn_mfma_f32_16x16x32_bf16(__builtin_bit_cast(v8b, a), __builtin_bit_cast(v8b, b), c, 0, 0, 0); }
template <int C> DEV float dpp(float x) { return __builtin_bit_cast(float, __builtin_amdgcn_mov_dpp(__builtin_bit_cast(int, x), C, 0xf, 0xf, true)); }
template <int C> DEV u32 dppu(u32 x) { return (u32)__builtin_amdgcn_mov_dpp((int)x, C, 0xf, 0xf, true); }
constexpr int X1 = 0xB1, X2 = 0x4E, X7 = 0x141, X15 = 0x140, ROR4 = 0x124, ROR8 = 0x128, SHL2 = 0x102, SHL4 = 0x104, SHL6 = 0x106;
DEV float add16(float a, float b) { auto s = __builtin_amdgcn_permlane16_swap(__float_as_uint(a), __float_as_uint(b), false, false); return __uint_as_float(s[0]) + __uint_as_float(s[1]); }
DEV float add32(float a, float b) { auto s = __builtin_amdgcn_permlane32_swap(__float_as_uint(a), __float_as_uint(b), false, false); return __uint_as_float(s[0]) + __uint_as_float(s[1]); }
DEV float mul32(float a, float b) { auto s = __builtin_amdgcn_permlane32_swap(__float_as_uint(a), __float_as_uint(b), false, false); return __uint_as_float(s[0]) * __uint_as_float(s[1]); }
DEV float max16(float a) { auto s = __builtin_amdgcn_permlane16_swap(__float_as_uint(a), __float_as_uint(a), false, false); return fmaxf(__uint_as_float(s[0]), __uint_as_float(s[1])); }
DEV float max32(float a) { auto s = __builtin_amdgcn_permlane32_swap(__float_as_uint(a), __float_as_uint(a), false, false); return fmaxf(__uint_as_float(s[0]), __uint_as_float(s[1])); }
DEV float row_sum(float x) { x += dpp<X1>(x); x += dpp<X2>(x); x += dpp<X7>(x); x += dpp<X15>(x); return x; }
DEV float wave_sum(float x) { x = row_sum(x); x = add16(x, x); return add32(x, x); }
DEV float wave_max(float x) { x = fmaxf(x, dpp<X1>(x)); x = fmaxf(x, dpp<X2>(x)); x = fmaxf(x, dpp<X7>(x)); x = fmaxf(x, dpp<X15>(x)); return max32(max16(x)); }
DEV float sel(bool c, float a, float b) { return c ? a : b; }
DEV u32 safe(u32 x) { return min(x, EMPTY - 1u); }
DEV u32 umax4(v4u v) { return max(max(v.x, v.y), max(v.z, v.w)); }
DEV v4u lr16(LAS char* p) { return *(LAS v4u*)p; }
DEV v4u trfrag(LAS char* p0, LAS char* p1) {
  const v4s a = __builtin_amdgcn_ds_read_tr16_b64_v4i16((LAS v4s*)p0), b = __builtin_amdgcn_ds_read_tr16_b64_v4i16((LAS v4s*)p1);
  const v2u x = __builtin_bit_cast(v2u, a), y = __builtin_bit_cast(v2u, b);
  return v4u{x.x, x.y, y.x, y.y};
}
DEV void bar() { asm volatile("s_waitcnt lgkmcnt(0)\n\ts_barrier" ::: "memory"); }
#define FENCE() do { asm volatile("" ::: "memory"); __builtin_amdgcn_sched_barrier(0); } while (0)

#define STAMP(i) do {} while (0)
#define STAMP_DECL do {} while (0)
#define STAMP_DUMP() do {} while (0)

DEV u32 ldsr(LAS char* p) { return __builtin_amdgcn_readfirstlane(*(volatile LAS u32*)p); }
DEV void lds_inc(LAS char* p, int lane) { if (lane == 0) __hip_atomic_fetch_add((LAS u32*)p, 1u, __ATOMIC_RELAXED, __HIP_MEMORY_SCOPE_WORKGROUP); }
DEV void lds_wait(LAS char* p, u32 v) {
#pragma nounroll
  for (int sp = 0; sp < SPIN_CAP; ++sp) { if (ldsr(p) == v) break; __builtin_amdgcn_s_sleep(8); }
  FENCE();
}
DEV v4u safe4(v4f o) { return v4u{safe(__float_as_uint(o.x)), safe(__float_as_uint(o.y)), safe(__float_as_uint(o.z)), safe(__float_as_uint(o.w))}; }
DEV bool full4(v4u a, v4u b, v4u c, v4u d) { return __all(max(max(umax4(a), umax4(b)), max(umax4(c), umax4(d))) != EMPTY); }

DEV u64 get_tag(v2u ep) { asm volatile("" : "+v"(ep)); return (((u64)(u32)__builtin_amdgcn_readfirstlane(ep.y) << 32) | (u32)__builtin_amdgcn_readfirstlane(ep.x)) + 1; }
DEV void wait_until(u32 base, u32 dt) { while ((int)(now() - base - dt) < 0) __builtin_amdgcn_s_sleep(1); }
DEV u32 check_flags(rsrc_t rws, u64 tag, u32 t0, int lane) {
  const u32 tlo = (u32)tag & 0xffu, thi = (u32)(tag >> 8);
  v2u f;
#pragma nounroll
  for (int sp = 0; sp < SPIN_CAP; ++sp) {
    f = ld8<AUX_SC1>(rws, lane * 8, WS_FLAG);
    if (__all(((f.y ^ thi) | ((f.x ^ tlo) & 0xffu)) == 0)) break;
    __builtin_amdgcn_s_sleep(64); FENCE();
  }
  if (!P_GLOB) return t0;
  const float d = fminf(fmaxf((float)((int)((f.x & ~0xffu) - (t0 << 8)) >> 8), -1000.f), 1000.f);
  return t0 + (u32)__builtin_amdgcn_readfirstlane((int)(wave_sum(d) * (1.f / 64.f)));
}

struct Args { const char *x, *K, *V, *wq, *wo, *wg, *wu, *wd; char *out, *ws; };

DEV void stream_wave(const Args& a, LAS char* L, int bh, int s, int w, int lane, u32 t0, u32 pf) {
  STAMP_DECL;
  const rsrc_t rk = mkrs(a.K + ((size_t)bh << 22), 1u << 22), rv = mkrs(a.V + ((size_t)bh << 22), 1u << 22);
  constexpr u32 PSTR = 16384;
  const int np = s < 4 ? NP : NP - 1;
  const u32 voff = lane * 16 + w * 4096 + (s * NP - (s == 5)) * PSTR;
  int lim = P_OFF + P_TAU;
  u32 tb = t0;
  auto pace = [&]() { while ((int)((now() - tb) << 6) < lim) __builtin_amdgcn_s_sleep(1); lim += P_TAU; };
  const u32 kb = (u32)(size_t)(L + L_KB) + w * 4096;
#pragma nounroll
  for (int t = 0; t < NL; ++t) {
    u32 keep;
    asm volatile("s_mov_b32 %0, m0\n\ts_mov_b32 m0, %1\n\t"
                 "s_waitcnt vmcnt(7)\n\tbuffer_load_dwordx4 %2, %3, %4 offen nt lds\n\t"
                 "s_waitcnt vmcnt(7)\n\tbuffer_load_dwordx4 %2, %3, %4 offen offset:1024 nt lds\n\t"
                 "s_waitcnt vmcnt(7)\n\tbuffer_load_dwordx4 %2, %3, %4 offen offset:2048 nt lds\n\t"
                 "s_waitcnt vmcnt(7)\n\tbuffer_load_dwordx4 %2, %3, %4 offen offset:3072 nt lds\n\t"
                 "s_mov_b32 m0, %0"
                 : "=&s"(keep) : "s"(kb + t * 16384), "v"(voff), "s"(rk), "s"(t * PSTR) : "memory");
    pace();
  }
  v4u buf[NPRE][4];
#pragma unroll
  for (int t = 0; t < NPRE; ++t) {
    if (t) pace();
#pragma unroll
    for (int i = 0; i < 4; ++i) {
      asm volatile("s_waitcnt vmcnt(7)" ::: "memory");
      buf[t][i] = ld16<AUX_NT>(rk, voff + i * 1024, (NL + t) * PSTR); FENCE();
    }
    if (w == 2) STAMP(32 + NL + t);
  }
  asm volatile("" :: "v"(pf));
  if (w == 0) STAMP(1);
  bar();
  if (w == 0) STAMP(2);
  const v4u q = lr16(L + L_Q + (lane & 31) * 16);
  LAS char* sc = L + L_SC + w * SCW;
  LAS char* scw = sc + (lane >> 5) * 16 + (((lane >> 4) & 1) + ((lane >> 2) & 2)) * 4;
  const bool hi8 = lane & 8, writer = (lane & 7) == 0;
  auto qk = [&](v4u k) { return dot2(k.w, q.w, dot2(k.z, q.z, dot2(k.y, q.y, dot2(k.x, q.x, 0.f)))); };
  auto reduce = [&](float d0, float d1, float d2, float d3, int t) {
    const float e0 = add16(d0, d1), e1 = add16(d2, d3);
    const float f0 = e0 + dpp<ROR8>(e0), f1 = e1 + dpp<ROR8>(e1);
    float f = hi8 ? f1 : f0;
    f += dpp<X7>(f); f += dpp<X2>(f); f += dpp<X1>(f);
    if (writer) *(LAS float*)(scw + t * 32) = f;
  };
  auto eatk = [&](int t) { reduce(qk(buf[t][0]), qk(buf[t][1]), qk(buf[t][2]), qk(buf[t][3]), NL + t); FENCE(); };
  auto refill = [&](int d) {
    pace();
#pragma unroll
    for (int i = 0; i < 4; ++i) { buf[d][i] = ld16<AUX_NT>(rk, voff + i * 1024, (NL + NPRE + d) * PSTR); FENCE(); }
  };
#pragma unroll
  for (int t = 0; t < NPRE; ++t) {
    eatk(t);
    if (t == (NPRE > 2 ? 2 : 0)) refill(0);
    if (t == (NPRE > 5 ? 5 : NPRE - 1)) refill(1);
  }
#pragma nounroll
  for (int t = NL + NPRE; t < NP; t += 2) {
    const rsrc_t rn = t < NP - 2 ? rk : rv;
    if (P_GLOB) tb = ldsr(L + L_CNT + 8);
#pragma unroll
    for (int d = 0; d < 2; ++d) {
      pace();
      float dd[4];
      const int u = t + d + 2;
      const u32 so = (u < NP ? u : u - NP) * PSTR, vo = (u >= np && u < NP) ? OOB : voff;
#pragma unroll
      for (int i = 0; i < 4; ++i) { dd[i] = qk(buf[d][i]); asm volatile("" : "+v"(dd[i])); buf[d][i] = ld16<AUX_NT>(rn, vo + i * 1024, so); FENCE(); }
      reduce(dd[0], dd[1], dd[2], dd[3], t + d); FENCE();
    }
    const int tl = (t - NL - NPRE) >> 1;
    if (tl < NL) {
      LAS char* p = L + L_KB + tl * 16384 + w * 4096 + lane * 16;
      reduce(qk(lr16(p)), qk(lr16(p + 1024)), qk(lr16(p + 2048)), qk(lr16(p + 3072)), tl); FENCE();
    }
  }
  static_assert((NP - NL - NPRE) / 2 >= NL, "the steady loop drains the LDS backlog");
  if (w == 0) STAMP(3);
  {
    constexpr float C2 = 0.0625f * 1.4426950408889634f;
    const v4f sa = *(LAS v4f*)(sc + lane * 16);
    const int l2 = lane < 2 * NP - 64 ? lane : 0;
    v4f sb = *(LAS v4f*)(sc + 1024 + l2 * 16);
    if (lane >= 2 * np - 64) sb = v4f{-INFINITY, -INFINITY, -INFINITY, -INFINITY};
    const float m = wave_max(fmaxf(fmaxf(fmaxf(sa.x, sa.y), fmaxf(sa.z, sa.w)), fmaxf(fmaxf(sb.x, sb.y), fmaxf(sb.z, sb.w)))) * C2;
    v4f pa, pb;
#pragma unroll
    for (int i = 0; i < 4; ++i) { pa[i] = __builtin_amdgcn_exp2f(fmaf(sa[i], C2, -m)); pb[i] = __builtin_amdgcn_exp2f(fmaf(sb[i], C2, -m)); }
    *(LAS v4u*)(sc + lane * 16) = v4u{pk(pa.x, pa.x), pk(pa.y, pa.y), pk(pa.z, pa.z), pk(pa.w, pa.w)};
    if (lane < 2 * NP - 64) *(LAS v4u*)(sc + 1024 + lane * 16) = v4u{pk(pb.x, pb.x), pk(pb.y, pb.y), pk(pb.z, pb.z), pk(pb.w, pb.w)};
    const float l = wave_sum(((pa.x + pa.y) + (pa.z + pa.w)) + ((pb.x + pb.y) + (pb.z + pb.w)));
    if (lane == 0) *(LAS v2f*)(L + L_ML + w * 8) = v2f{m, l};
    asm volatile("s_waitcnt lgkmcnt(0)" ::: "memory");
    lds_inc(L + L_CNT + 4, lane);
  }
  v4u msk;
  {
    const int g = lane >> 4, i = lane & 15;
    const u32 mm = ((i >> 3) == (g & 1)) ? ((i & 1) ? 0xffff0000u : 0x0000ffffu) : 0u;
    const int t = (i & 7) >> 1;
    msk = v4u{t == 0 ? mm : 0u, t == 1 ? mm : 0u, t == 2 ? mm : 0u, t == 3 ? mm : 0u};
  }
  v4f acc = {0.f, 0.f, 0.f, 0.f};
  LAS char* pr = sc + (lane >> 5) * 16;
  LAS char* accw = L + L_ACC + w * 1024 + ((lane >> 5) * 16 + (lane & 15)) * 32 + ((lane >> 4) & 1) * 16;
  asm volatile("" : "+v"(accw));
  auto eatv = [&](int d, int t) {
    const v4u p = lr16(pr + t * 32);
    if (t + 2 < np) pace();
    const u32 so = (t + 2) * PSTR, vo = t + 2 < np ? voff : OOB;
#pragma unroll
    for (int i = 0; i < 4; ++i) {
      acc = mfma(msk & p[i], buf[d][i], acc); asm volatile("" : "+v"(acc));
      buf[d][i] = ld16<AUX_NT>(rv, vo + i * 1024, so); FENCE();
    }
  };
  eatv(0, 0);
#pragma nounroll
  for (int t = 1; t < NP; t += 2) { eatv(1, t); eatv(0, t + 1); }
  *(LAS v4f*)accw = acc;
  if (w == 0) STAMP(4);
  bar();
}

DEV void stream_helper(const Args& a, LAS char* L, int bh, int s, int lane, u32 t0) {
  STAMP_DECL;
  const rsrc_t rws = mkrs(a.ws, WS_END);
  const v2u ep = ld8<AUX_SC1>(rws, WS_EPOCH, 0);
  if (lane == 0) *(LAS v4u*)(L + L_CNT) = v4u{0u, 0u, t0, 0u};
  const u64 tag = get_tag(ep);
  {
    const u32 tlo = (u32)tag << 16, thi = (u32)(tag >> 16);
    wait_until(t0, P_QT);
    v4u g0, g1;
#pragma nounroll
    for (int sp = 0; sp < SPIN_CAP; ++sp) {
      g0 = ld16<AUX_SC1>(rws, lane * 16, WS_Q + bh * 2048); g1 = ld16<AUX_SC1>(rws, lane * 16, WS_Q + bh * 2048 + 1024);
      const u32 bad = (g0.y ^ thi) | (g0.w ^ thi) | (g1.y ^ thi) | (g1.w ^ thi) | (((g0.x ^ tlo) | (g0.z ^ tlo) | (g1.x ^ tlo) | (g1.z ^ tlo)) >> 16);
      if (__all(bad == 0)) break;
      __builtin_amdgcn_s_sleep(P_QDELAY); FENCE();
    }
    *(LAS u32*)(L + L_Q + lane * 4) = (g0.x & 0xffffu) | (g0.z << 16);
    *(LAS u32*)(L + L_Q + 256 + lane * 4) = (g1.x & 0xffffu) | (g1.z << 16);
    STAMP(10);
  }
  bar();
  float fac[4] = {0.f, 0.f, 0.f, 0.f};
  int pass = P_DRY ? 0 : 1;
  asm volatile("" : "+s"(pass));
#pragma nounroll
  for (; pass < 2; ++pass) {
    if (pass) {
      wait_until(t0, P_FLAGT);
      const u32 tb = check_flags(rws, tag, t0, lane);
      if (P_GLOB && lane == 0) *(LAS u32*)(L + L_CNT + 8) = tb;
      STAMP(12);
      lds_wait(L + L_CNT + 4, 4u);
      st16<AUX_SC1>(safe4(*(LAS v4f*)(L + L_ML + (lane & 1) * 16)), rws, lane < 2 ? lane * 16 : OOB, WS_ML + bh * 256 + s * 32);
      v2u ml;
#pragma nounroll
      for (int sp = 0; sp < SPIN_CAP; ++sp) {
        __builtin_amdgcn_s_sleep(127); FENCE();
        ml = ld8<AUX_SC1>(rws, lane < 24 ? lane * 8 : OOB, WS_ML + bh * 256);
        if (__all(max(ml.x, ml.y) != EMPTY)) break;
      }
      const float m = lane < 24 ? __uint_as_float(ml.x) : -INFINITY, M = wave_max(m), e = __builtin_amdgcn_exp2f(m - M);
      const float f = e / wave_sum(e * __uint_as_float(ml.y));
#pragma unroll
      for (int i = 0; i < 4; ++i) fac[i] = __uint_as_float(__builtin_amdgcn_readlane(__float_as_uint(f), s * 4 + i));
      STAMP(14);
      bar();
      STAMP(16);
    }
    const v4f a0 = *(LAS v4f*)(L + L_ACC + lane * 16), a1 = *(LAS v4f*)(L + L_ACC + 1024 + lane * 16);
    const v4f a2 = *(LAS v4f*)(L + L_ACC + 2048 + lane * 16), a3 = *(LAS v4f*)(L + L_ACC + 3072 + lane * 16);
    const v4f o = (a0 * fac[0] + a1 * fac[1]) + (a2 * fac[2] + a3 * fac[3]);
    st8<AUX_SC1>(v2u{safe(pk(o.x, o.y)), safe(pk(o.z, o.w))}, rws, pass ? lane * 8 : OOB, WS_O + (bh & 15) * 6144 + ((bh >> 4) * 6 + s) * 512);
  }
  STAMP(17);
  STAMP_DUMP();
}

DEV void service_wave(const Args& a, LAS char* L, int x, int j, int w, int lane, u32 t0, u32 pf) {
  STAMP_DECL;
  const int g = lane >> 4, n = lane & 15, hw = w & 3, fj = x * 7 + j;
  const rsrc_t rws = mkrs(a.ws, WS_END), rx = mkrs(a.x, 2 * D * 2);
  constexpr u32 ONES = 0x3F803F80u;
  const v4f zf = {0.f, 0.f, 0.f, 0.f};
  if (w == 0) STAMP(30);
  const int cgp = P_QLOCAL ? x * 8 + j : j * 8 + x;
  const v4u xr = ld16<0>(rx, lane < 56 ? lane * 16 : OOB, (w & 1) * (D * 2));
  FENCE();
  {
    const rsrc_t rq = mkrs(a.wq, D * HD * 2);
#pragma unroll
    for (int i = 0; i < 7; ++i) dma16<AUX_NT>(rq, L + L_WQ + (i * 8 + w) * 1024, (P_ABL & 1) ? OOB : (lane >> 3) * (HD * 2) + (lane & 7) * 16 + cgp * 128, (i * 8 + w) * 8 * HD * 2);
  }
  FENCE();
  const v2u ep = ld8<AUX_SC1>(rws, WS_EPOCH, 0);
  FENCE();
  if (w == 0) STAMP(31);
  if (w < 2) {
    float v[8] = {lo16(xr.x), hi16(xr.x), lo16(xr.y), hi16(xr.y), lo16(xr.z), hi16(xr.z), lo16(xr.w), hi16(xr.w)};
    const float mean = wave_sum(((v[0] + v[1]) + (v[2] + v[3])) + ((v[4] + v[5]) + (v[6] + v[7]))) * (1.f / D);
    float ss = 0.f;
#pragma unroll
    for (int i = 0; i < 8; ++i) { v[i] -= mean; ss = fmaf(v[i], v[i], ss); }
    const float rs = __builtin_amdgcn_rsqf(wave_sum(lane < 56 ? ss : 0.f) * (1.f / D) + 1e-5f);
    if (lane < 56) *(LAS v4u*)(L + L_XN + w * (D * 2) + lane * 16) = v4u{pk(v[0] * rs, v[1] * rs), pk(v[2] * rs, v[3] * rs), pk(v[4] * rs, v[5] * rs), pk(v[6] * rs, v[7] * rs)};
  }
  if (w == 2 && lane < 4) *(LAS u32*)(L + L_ZERO + lane * 4) = 0u;
  asm volatile("s_waitcnt vmcnt(0)" :: "v"(pf), "v"(xr) : "memory");
  bar();
  if (w == 0) STAMP(8);
  const u64 tag = get_tag(ep);
  if (w < 4) {
    v4f qa = zf;
#pragma unroll
    for (int t = 0; t < 14; ++t) {
      LAS char* p = L + L_WQ + (t * 32 + g * 8 + (n >> 2)) * 128 + w * 32 + (n & 3) * 8;
      qa = mfma(lr16(L + L_XN + (n & 1) * (D * 2) + t * 64 + g * 16), trfrag(p, p + 512), qa);
    }
    const float qv = (g & 1) ? qa.y : qa.x;
    st8<AUX_SC1>(v2u{((u32)tag << 16) | (pk(qv, qv) & 0xffffu), (u32)(tag >> 16)}, rws, g < 2 ? g * (HD * 8) + n * 8 : OOB, WS_Q + (cgp * 64 + w * 16) * 8);
    if (w == 0) STAMP(9);
  } else {
    const v4u e4 = {EMPTY, EMPTY, EMPTY, EMPTY};
    const u32 co = WS_CLR + (x * 8 + j) * CLR_PER_WG + hw * 1536;
    if (P_ABL & 8) wait_until(t0, 900);
    st16<AUX_SC1>(e4, rws, lane * 16, co);
    st16<AUX_SC1>(e4, rws, lane < 32 ? 1024 + lane * 16 : OOB, co);
    asm volatile("s_waitcnt vmcnt(0)" ::: "memory");
  }
  bar();
  if (w == 7) { st8<AUX_SC1>(v2u{(t0 << 8) | ((u32)tag & 0xffu), (u32)(tag >> 8)}, rws, lane == 0 ? WS_FLAG + (x * 8 + j) * 8 : OOB, 0); STAMP(11); }
  if (j == 7) { STAMP_DUMP(); return; }

  int pass = P_DRY ? 0 : 1;
  asm volatile("" : "+s"(pass));
#pragma nounroll
  for (; pass < 2; ++pass) {
    if (pass) {
      if (w == 7) {
        wait_until(t0, P_FLAGT);
        const u32 tb = check_flags(rws, tag, t0, lane);
        if (lane == 0) *(LAS u32*)(L + L_TB) = tb;
      } else {
        const rsrc_t rw = mkrs(w < 4 ? a.wo : w == 4 ? a.wg : w == 5 ? a.wu : a.wd, w < 4 ? HD * D * 2 : D * F * 2);
        const u32 vo = w < 4 ? (x * 512 + w * 128 + (lane >> 3)) * (D * 2) + (lane & 7) * 16 + j * 128
                     : w == 6 ? lane * 16 + fj * (32 * D * 2) : (lane >> 2) * (F * 2) + (lane & 3) * 16 + fj * 64;
        const u32 st = w < 4 ? 8 * D * 2 : w == 6 ? 1024 : 16 * F * 2;
        LAS char* dst = L + (w < 4 ? L_WO + w * 16384 : L_WG + hw * 28672);
        const int cnt = P_NOW ? 0 : w < 4 ? 16 : 28;
        const u32 step = w < 4 ? P_WSPAN / 16 : P_WSPAN / 28;
        u32 due = P_WBEG + hw * step / 4;
#pragma nounroll
        for (int i = 0; i < cnt; ++i) {
          while (now() - t0 < due) __builtin_amdgcn_s_sleep(4);
          if (w == 4 || w == 5) dma16<P_WAUX>(rw, dst + i * 1024, vo, i * st); else dma16<AUX_NT>(rw, dst + i * 1024, vo, i * st);
          due += step;
        }
        asm volatile("s_waitcnt vmcnt(0)" ::: "memory");
      }
      bar();
      if (w == 0) STAMP(13);
    }
    const rsrc_t rt = mkrs(a.ws, pass ? WS_END : 0u);
    if (w < 4) {
      v4u wf[4][4];
#pragma unroll
      for (int t = 0; t < 4; ++t)
#pragma unroll
        for (int c = 0; c < 4; ++c) { LAS char* p = L + L_WO + w * 16384 + (t * 32 + g * 8 + (n >> 2)) * 128 + c * 32 + (n & 3) * 8; wf[t][c] = trfrag(p, p + 512); }
      LAS char* tr = L + L_WO + w * 16384;
      const int prow = (n >> 3) * 6 + (n & 7);
      LAS char* tw[3]; LAS char* td[4];
#pragma unroll
      for (int k = 0; k < 3; ++k) tw[k] = tr + (k * 4 + g) * 256 + ((n ^ (k * 4 + g)) * 16);
#pragma unroll
      for (int t = 0; t < 4; ++t) td[t] = (n & 7) < 6 ? tr + prow * 256 + (((t * 4 + g) ^ prow) * 16) : L + L_ZERO;
      LAS char* yw = L + L_YR + w * 512 + (lane >> 5) * 256 + (lane & 31) * 4;
      asm volatile("s_waitcnt lgkmcnt(0)" : "+v"(tw[0]), "+v"(tw[1]), "+v"(tw[2]), "+v"(td[0]), "+v"(td[1]), "+v"(td[2]), "+v"(td[3]), "+v"(yw) :: "memory");
      if (pass) wait_until(ldsr(L + L_TB), LAST_ISSUE + P_T1);
      if (pass && w == 2) STAMP(15);
      v4u o[3];
#pragma nounroll
      for (int sp = 0; sp < SPIN_CAP; ++sp) {
#pragma unroll
        for (int k = 0; k < 3; ++k) o[k] = ld16<AUX_SC1>(rt, g * 512 + n * 16, WS_O + (x * 2 + (w >> 1)) * 6144 + (w & 1) * 256 + k * 2048);
        FENCE();
        if (full4(o[0], o[1], o[2], o[2])) break;
        FENCE();
      }
      if (pass && w == 2) STAMP(18);
#pragma unroll
      for (int k = 0; k < 3; ++k) *(LAS v4u*)tw[k] = o[k];
      v4u af[4];
#pragma unroll
      for (int t = 0; t < 4; ++t) af[t] = lr16(td[t]);
      FENCE();
      v4f y[4] = {zf, zf, zf, zf};
#pragma unroll
      for (int c = 0; c < 4; ++c)
#pragma unroll
        for (int t = 0; t < 4; ++t) y[c] = mfma(af[t], wf[t][c], y[c]);
      *(LAS float*)yw = add16((y[0].x + y[0].y) + (y[0].z + y[0].w), (y[1].x + y[1].y) + (y[1].z + y[1].w));
      *(LAS float*)(yw + 128) = add16((y[2].x + y[2].y) + (y[2].z + y[2].w), (y[3].x + y[3].y) + (y[3].z + y[3].w));
      if (pass) bar();
      const v2f u = (*(LAS v2f*)(L + L_YR + lane * 8) + *(LAS v2f*)(L + L_YR + 512 + lane * 8)) + (*(LAS v2f*)(L + L_YR + 1024 + lane * 8) + *(LAS v2f*)(L + L_YR + 1536 + lane * 8));
      __builtin_amdgcn_raw_buffer_store_b32(safe(pk(u.x, u.y)), rt, w < P_YR ? (lane >> 5) * (D * 2) + (lane & 31) * 4 : OOB, WS_Y + w * YSTR + x * 1792 + j * 128, AUX_SC1);
      if (pass && w == 0) STAMP(19);
    } else {
      v4u guf[14], wdf[7];
      v4f cs = zf;
#pragma unroll
      for (int t = 0; t < 14; ++t) {
        LAS char* p = L + L_WG + ((n >> 1) & 1) * 28672 + (t * 32 + g * 8 + (n >> 2)) * 64 + hw * 16 + (n & 1) * 8;
        guf[t] = trfrag(p, p + 256);
        cs = mfma(guf[t], v4u{ONES, ONES, ONES, ONES}, cs);
      }
#pragma unroll
      for (int t = 0; t < 7; ++t) { LAS char* p = L + L_WD + (g * 8 + (n >> 2)) * (D * 2) + (hw * 7 + t) * 32 + (n & 3) * 8; wdf[t] = trfrag(p, p + 4 * D * 2); }
      const int hv = (hw * 32 + (g >> 1) * 16 + n) * 8 + (g & 1) * 4;
      const v2u xx = ld8<0>(rx, hv * 2, 0);
      v4f xc = {lo16(xx.x), hi16(xx.x), lo16(xx.y), hi16(xx.y)};
      v4u sel0, sel1;
      {
        const u32 one = (n & 1) ? 0x3F800000u : 0x00003F80u;
        const int t = (n & 7) >> 1;
        const v4u d = {t == 0 ? one : 0u, t == 1 ? one : 0u, t == 2 ? one : 0u, t == 3 ? one : 0u}, z = {0u, 0u, 0u, 0u};
        sel0 = n < 8 ? d : z; sel1 = n < 8 ? z : d;
      }
      const int tA = hw * 7 + g, tB = hw * 7 + 4 + g;
      LAS char* hA = L + (tA == fj ? L_HF + (tA * 16 + n) * 4 : L_ZERO);
      LAS char* hB = L + (tB == fj && g < 3 ? L_HF + (tB * 16 + n) * 4 : L_ZERO);
      const int hAs = tA == fj ? D * 4 : 0, hBs = tB == fj && g < 3 ? D * 4 : 0;
      LAS char* ga[4];
#pragma unroll
      for (int v = 0; v < 4; ++v) ga[v] = L + ((n >> 2) == v && (n & 2) == 0 ? L_GU + (n & 1) * 64 + g * 16 : L_ZERO);
      const u32 yo0 = g * 1792 + (hw * 32 + n) * 16, yo1 = hw < 3 ? yo0 + 256 : OOB, ys = WS_Y + (x % P_YR) * YSTR;
      const u32 fo0 = tA * FSTR + n * 8, fo1 = g < 3 ? tB * FSTR + n * 8 : OOB;
      const u32 fr = (lane >> 2) * 128 + (lane & 3) * 16, fr3 = lane < 32 ? fr : OOB;
      asm volatile("" : "+v"(cs), "+v"(xc));
      if (pass) bar();
      __builtin_amdgcn_s_sleep(P_D2);
      v4u yl[4];
#pragma nounroll
      for (int sp = 0; sp < SPIN_CAP; ++sp) {
        yl[0] = ld16<AUX_SC1>(rt, yo0, ys); yl[1] = ld16<AUX_SC1>(rt, yo0, ys + 4 * 1792);
        yl[2] = ld16<AUX_SC1>(rt, yo1, ys); yl[3] = ld16<AUX_SC1>(rt, yo1, ys + 4 * 1792);
        FENCE();
        if (full4(yl[0], yl[1], yl[2], yl[3])) break;
        FENCE();
      }
      if (pass && hw == 2) STAMP(20);
      v4f h = mfma(sel0, yl[0], xc);
      h = mfma(sel0, yl[1], h); h = mfma(sel1, yl[2], h); h = mfma(sel1, yl[3], h);
      if (hv < 2 * D) { *(LAS v2u*)(L + L_HB + hv * 2) = v2u{pk(h.x, h.y), pk(h.z, h.w)}; *(LAS v4f*)(L + L_HF + hv * 4) = h; }
      if (pass) bar();
      if (pass && hw == 2) STAMP(21);
      v4u hf[14];
#pragma unroll
      for (int t = 0; t < 4; ++t) hf[t] = lr16(L + L_HB + (n & 1) * (D * 2) + t * 64 + g * 16);
      const v4u h0 = lr16(lane < 56 ? L + L_HB + lane * 16 : L + L_ZERO), h1 = lr16(lane < 56 ? L + L_HB + D * 2 + lane * 16 : L + L_ZERO);
#pragma unroll
      for (int t = 4; t < 14; ++t) hf[t] = lr16(L + L_HB + (n & 1) * (D * 2) + t * 64 + g * 16);
      FENCE();
      v4f dA = {*(LAS float*)hA, *(LAS float*)(hA + hAs), 0.f, 0.f}, dB = {*(LAS float*)hB, *(LAS float*)(hB + hBs), 0.f, 0.f};
      FENCE();
      v4f gu = zf;
#pragma unroll
      for (int t = 0; t < 14; ++t) gu = mfma(guf[t], hf[t], gu);
      float s0 = 0.f, s1 = 0.f, q0 = 0.f, q1 = 0.f;
#pragma unroll
      for (int i = 0; i < 4; ++i) { s0 = dot2(h0[i], ONES, s0); s1 = dot2(h1[i], ONES, s1); q0 = dot2(h0[i], h0[i], q0); q1 = dot2(h1[i], h1[i], q1); }
      float st = add16(add32(s0, s1), add32(q0, q1));
      st = row_sum(st) * (1.f / D);
      const float m0 = __uint_as_float(__builtin_amdgcn_readlane(__float_as_uint(st), 0)), e0 = __uint_as_float(__builtin_amdgcn_readlane(__float_as_uint(st), 16));
      const float m1 = __uint_as_float(__builtin_amdgcn_readlane(__float_as_uint(st), 32)), e1 = __uint_as_float(__builtin_amdgcn_readlane(__float_as_uint(st), 48));
      const float mean = (n & 1) ? m1 : m0, ex2 = (n & 1) ? e1 : e0;
      const float r2 = __builtin_amdgcn_rcpf(fmaxf(fmaf(-mean, mean, ex2), 0.f) + 1e-5f);
#pragma unroll
      for (int i = 0; i < 14; ++i) { __builtin_amdgcn_sched_group_barrier(0x8, 1, 0); __builtin_amdgcn_sched_group_barrier(0x2, 3, 0); }
      const v4f c = gu - mean * cs;
      const u32 gd = pk(mul32(c.x, c.z) * r2, mul32(c.y, c.w) * r2);
      if (n < 2) *(LAS u32*)(L + L_GU + n * 64 + hw * 16 + (g & 1) * 8 + (g >> 1) * 4) = gd;
      if (pass) bar();
      if (pass && hw == 2) STAMP(22);
      const v4u g0 = lr16(ga[0]), g1 = lr16(ga[1]), g2 = lr16(ga[2]), g3 = lr16(ga[3]);
      dA = mfma(g0, wdf[0], dA); dA = mfma(g1, wdf[1], dA); dA = mfma(g2, wdf[2], dA); dA = mfma(g3, wdf[3], dA);
      dB = mfma(g0, wdf[4], dB); dB = mfma(g1, wdf[5], dB); dB = mfma(g2, wdf[6], dB);
      st8<AUX_SC1>(v2u{safe(__float_as_uint(dA.x)), safe(__float_as_uint(dA.y))}, rt, fo0, WS_F + fj * 128);
      st8<AUX_SC1>(v2u{safe(__float_as_uint(dB.x)), safe(__float_as_uint(dB.y))}, rt, fo1, WS_F + fj * 128);
      if (pass && hw == 2) STAMP(23);
      if (hw == 1) {
        __builtin_amdgcn_s_sleep(P_D3);
        v4u f[4];
#pragma nounroll
        for (int sp = 0; sp < SPIN_CAP; ++sp) {
#pragma unroll
          for (int i = 0; i < 4; ++i) f[i] = ld16<AUX_SC1>(rt, (i < 3 ? fr : fr3) + (i & 1) * 2048, WS_F + (fj >> 1) * FSTR + (fj & 1) * 64 + (i >> 1) * 4096);
          FENCE();
          if (full4(f[0], f[1], f[2], f[3])) break;
          FENCE();
        }
        if (pass) STAMP(24);
        const v4f t = (__builtin_bit_cast(v4f, f[0]) + __builtin_bit_cast(v4f, f[1])) + (__builtin_bit_cast(v4f, f[2]) + __builtin_bit_cast(v4f, f[3]));
        float o = add16(add32(t.x, t.y), add32(t.z, t.w));
        o += dpp<ROR4>(o); o += dpp<ROR8>(o);
        __builtin_amdgcn_raw_buffer_store_b16((unsigned short)pk(o, o), mkrs(a.out, pass ? 2 * D * 2 : 0), n < 4 ? (g >> 1) * (D * 2) + (n * 2 + (g & 1)) * 2 : OOB, fj * 16, 0);
        st8<AUX_SC1>(v2u{(u32)tag, (u32)(tag >> 32)}, rt, (lane == 0 && fj == 0) ? WS_EPOCH : OOB, 0);
        if (pass) STAMP(25);
      }
    }
  }
  STAMP_DUMP();
}

__global__ void __launch_bounds__(512, 2) fused_decode(Args a) {
  extern __shared__ __attribute__((aligned(16))) char smem[];
  LAS char* L = (LAS char*)smem;
  const u32 t0 = now();
  u32 pf = 0;
  u64 pc;
  asm volatile("s_getpc_b64 %0" : "=s"(pc));
  pf = __builtin_amdgcn_raw_buffer_load_b32(mkrs((const char*)(pc & ~255ull), P_CODEPF_BYTES), threadIdx.x * 32, 0, 0);
  FENCE();
  asm volatile("" :: "s"(a.x), "s"(a.K), "s"(a.V), "s"(a.wq), "s"(a.ws));
  const int lane = threadIdx.x & 63, w = __builtin_amdgcn_readfirstlane(threadIdx.x >> 6);
  const int x = blockIdx.x & 7, r = blockIdx.x >> 3;
  if (r >= 8) {
    const int id = P_MAP ? r - 8 : blockIdx.x - 64, qd = id / 6, s = id - qd * 6;
    const int bh = P_MAP ? (qd >> 1) * 16 + x * 2 + (qd & 1) : qd;
    if (w < 4) stream_wave(a, L, bh, s, w, lane, t0, pf);
    else { asm volatile("" :: "v"(pf)); if (w == 4) stream_helper(a, L, bh, s, lane, t0); }
  } else {
    service_wave(a, L, x, r, w, lane, t0, pf);
  }
}

extern "C" void kernel_launch(void* const* d_in, const int* in_sizes, int n_in, void* d_out, int out_size, void* d_ws, size_t ws_size, hipStream_t stream) {
  Args a;
  a.x = (const char*)d_in[0]; a.K = (const char*)d_in[1]; a.V = (const char*)d_in[2]; a.wq = (const char*)d_in[3]; a.wo = (const char*)d_in[4];
  a.wg = (const char*)d_in[6]; a.wu = (const char*)d_in[7]; a.wd = (const char*)d_in[8];
  a.out = (char*)d_out; a.ws = (char*)d_ws;
  (void)hipFuncSetAttribute((const void*)fused_decode, hipFuncAttributeMaxDynamicSharedMemorySize, L_END);
  fused_decode<<<256, 512, L_END, stream>>>(a);
}
```

```cpp
#include <hip/hip_runtime.h>
#include <cstdint>

typedef unsigned u32;
typedef unsigned long long u64;
typedef u32 v2u __attribute__((ext_vector_type(2)));
typedef u32 v4u __attribute__((ext_vector_type(4)));
typedef u32 v16u __attribute__((ext_vector_type(16)));
typedef float v2f __attribute__((ext_vector_type(2)));
typedef float v4f __attribute__((ext_vector_type(4)));
typedef short v4s __attribute__((ext_vector_type(4)));
typedef __bf16 v2b __attribute__((ext_vector_type(2)));
typedef __bf16 v8b __attribute__((ext_vector_type(8)));
typedef __amdgpu_buffer_rsrc_t rsrc_t;
#define LAS __attribute__((address_space(3)))
#define DEV __device__ __forceinline__

constexpr int D = 448, F = 1792, HD = 4096;
constexpr int NP = 43;
constexpr int NL = 9, NR = 4;
constexpr int NE = NL + NR;
static_assert((NP - NE) % 3 == 0 && (NP - 1) % 3 == 0 && NR <= NL, "the steady loops handle three pieces per trip");
constexpr u32 PSTR = 6 * 16384;

constexpr int TAU = 2571;
constexpr int OFF = 600;
constexpr int TW = 3150;
constexpr int XT = 15;
constexpr int N1 = 17;
constexpr int WSTEP = 500;
constexpr int PROBE_TH = 84;
constexpr int PROBE_NOM = 70;
constexpr int SLEW = 40;
constexpr int FLAG_T = 400;
constexpr int Q_T = 590;
constexpr int C0 = 860;
constexpr int CPER = 258;
constexpr int CN = 10;
constexpr int CWIN = 45;
constexpr int CRD = 125;
constexpr int CMIN = 8;
constexpr int CADD = 20;
constexpr int WD_T = 40;
constexpr int ML_T = 108;
constexpr int O_T = 132;
constexpr int Y_D = 22;
constexpr int F_D = 23;
constexpr int TAU1 = TAU + XT * 64 / N1;
constexpr int sched(int t) { return OFF + (t >= 2 ? 3 * TW : 0) + (t < N1 ? t * TAU1 : N1 * TAU1 + (t - N1) * TAU); }
constexpr int WARM0 = sched(2) - 3 * TW, PULL0 = WARM0 + 2 * TW + TW / 4 + 640;
constexpr u32 LAST = (sched(2 * NP - 1) + TAU / 3) / 64;
constexpr u32 TIMEOUT = 200000000u;

constexpr int AUX_NT = 2, AUX_SC1 = 16;
constexpr u32 OOB = 0x80000000u, EMPTY = 0xFFFFFFFFu, ONES = 0x3F803F80u;

constexpr u32 WS_EPOCH = 0, WS_FLAG = 4096, WS_Q = 8192, WS_ML = WS_Q + 16384, WS_O = WS_ML + 192 * 128, WS_Y = WS_O + 192 * 512;
constexpr u32 YSTR = 8 * 1792, WS_F = WS_Y + 4 * YSTR, FSTR = 7424, WS_CTL = WS_F + 28 * FSTR;
constexpr u32 CLR = 6400, WS_END = WS_ML + 64 * CLR;
static_assert(WS_CTL % 128 == 0 && WS_CTL + CN * 512 <= WS_END, "the cleared region covers every inbox");

constexpr int L_WQ = 0, L_WO = 0, L_WG = 65536, L_WD = L_WG + 2 * 28672, L_XN = L_WD + 28672, L_HB = L_XN, L_HF = L_XN + 1792;
constexpr int L_GU = L_HF + 3584, L_ZERO = L_GU + 128, L_TB = L_ZERO + 16, L_R2 = L_TB + 8, L_YR = L_TB + 16;
constexpr int L_Q = 0, L_ML = 512, L_EM = 544, L_QOK = 576, L_TG = 580, L_CD = 584, L_CE = 588, L_CR = 640;
constexpr int L_SC = 1024, SCW = 1408, L_ACC = L_SC + 4 * SCW, L_SJ = L_ACC + 4096, L_KB = 12544;
constexpr int L_JUNK = L_KB + NL * 16384, L_SH = L_JUNK + 256, L_END = L_SH + 16;
static_assert(L_SJ + 256 + NP * 32 <= L_KB && L_YR + 2048 <= L_JUNK && L_YR % 16 == 0 && L_END <= 163840, "LDS maps");

struct Args { const char *x, *K, *V, *wq, *wo, *wg, *wu, *wd; char *out, *ws; };

DEV rsrc_t mkrs(const void* p, u32 n) { return __builtin_amdgcn_make_buffer_rsrc((void*)p, 0, n, 0x00020000); }
template <int AUX> DEV u32 ld4(rsrc_t r, u32 v, u32 s) { return __builtin_amdgcn_raw_buffer_load_b32(r, v, s, AUX); }
template <int AUX> DEV v2u ld8(rsrc_t r, u32 v, u32 s) { return __builtin_amdgcn_raw_buffer_load_b64(r, v, s, AUX); }
template <int AUX> DEV v4u ld16(rsrc_t r, u32 v, u32 s) { return __builtin_amdgcn_raw_buffer_load_b128(r, v, s, AUX); }
DEV void put4(u32 x, rsrc_t r, u32 v, u32 s) { __builtin_amdgcn_raw_buffer_store_b32(x, r, v, s, AUX_SC1); }
DEV void put8(v2u x, rsrc_t r, u32 v, u32 s) { __builtin_amdgcn_raw_buffer_store_b64(x, r, v, s, AUX_SC1); }
DEV void put16(v4u x, rsrc_t r, u32 v, u32 s) { __builtin_amdgcn_raw_buffer_store_b128(x, r, v, s, AUX_SC1); }
DEV u32 laddr(LAS char* p) { return (u32)(size_t)p; }

#define DMA(op, mods, rs, dst, vo, so)                                                                                  \
  do {                                                                                                                  \
    u32 keep_;                                                                                                          \
    asm volatile("s_mov_b32 %0, m0\n\ts_mov_b32 m0, %1\n\ts_nop 2\n\t" op " %2, %3, %4 offen " mods " lds\n\ts_mov_b32 m0, %0" \
                 : "=&s"(keep_) : "s"(dst), "v"(vo), "s"(rs), "s"(so) : "memory");                                      \
  } while (0)
#define WINDOW "s_waitcnt vmcnt(11)"
DEV void dma_piece(rsrc_t rs, u32 dst, u32 vo, u32 so) {
  u32 keep;
  asm volatile("s_mov_b32 %0, m0\n\ts_mov_b32 m0, %1\n\ts_nop 1\n\t"
               WINDOW "\n\tbuffer_load_dwordx4 %2, %3, %4 offen nt lds\n\t"
               WINDOW "\n\tbuffer_load_dwordx4 %2, %3, %4 offen offset:1024 nt lds\n\t"
               WINDOW "\n\tbuffer_load_dwordx4 %2, %3, %4 offen offset:2048 nt lds\n\t"
               WINDOW "\n\tbuffer_load_dwordx4 %2, %3, %4 offen offset:3072 nt lds\n\t"
               "s_mov_b32 m0, %0"
               : "=&s"(keep) : "s"(dst), "v"(vo), "s"(rs), "s"(so) : "memory");
}

#define FENCE() do { asm volatile("" ::: "memory"); __builtin_amdgcn_sched_barrier(0); } while (0)
#define VMWAIT(n) asm volatile("s_waitcnt vmcnt(" #n ")" ::: "memory")
#define SETTLE(n) do { __builtin_amdgcn_s_waitcnt(0); asm volatile("s_nop " #n ::: "memory"); } while (0)
#define ROLE_END(n) do { __builtin_amdgcn_s_waitcnt(0); asm volatile("s_endpgm ; role " #n ::: "memory"); } while (0)
#define INTERLEAVE(cnt, valu) _Pragma("unroll") for (int i_ = 0; i_ < cnt; ++i_) { __builtin_amdgcn_sched_group_barrier(0x8, 1, 0); __builtin_amdgcn_sched_group_barrier(0x2, valu, 0); }
DEV void bar() { __builtin_amdgcn_sched_barrier(0); asm volatile("s_waitcnt lgkmcnt(0)\n\ts_barrier" ::: "memory"); __builtin_amdgcn_sched_barrier(0); }

DEV u32 now() { return (u32)__builtin_amdgcn_s_memrealtime(); }
DEV int fine(u32 base) { return min((int)(now() - base), 1 << 24) * 64; }
DEV void wait_until(u32 t) { while ((int)(now() - t) < 0) __builtin_amdgcn_s_sleep(1); }
DEV void spin_until(u32 t) { while ((int)(now() - t) < 0) {} }
DEV bool expired(u32 t0) { return (int)(now() - t0) > (int)TIMEOUT; }

DEV float asf(u32 x) { return __uint_as_float(x); }
DEV u32 asu(float x) { return __float_as_uint(x); }
DEV float dot2(u32 a, u32 b, float c) { return __builtin_amdgcn_fdot2_f32_bf16(__builtin_bit_cast(v2b, a), __builtin_bit_cast(v2b, b), c, false); }
DEV u32 pk(float lo, float hi) { return __builtin_bit_cast(u32, __builtin_convertvector((v2f{lo, hi}), v2b)); }
DEV float lo16(u32 x) { return asf(x << 16); }
DEV float hi16(u32 x) { return asf(x & 0xffff0000u); }
DEV float rbf(float x) { return hi16(pk(x, x)); }
DEV v4f mfma(v4u a, v4u b, v4f c) { return __builtin_amdgcn_mfma_f32_16x16x32_bf16(__builtin_bit_cast(v8b, a), __builtin_bit_cast(v8b, b), c, 0, 0, 0); }
template <int C> DEV u32 dppu(u32 x) { return (u32)__builtin_amdgcn_mov_dpp((int)x, C, 0xf, 0xf, true); }
template <int C> DEV float dpp(float x) { return asf(dppu<C>(asu(x))); }
constexpr int X1 = 0xB1, X2 = 0x4E, X7 = 0x141, X15 = 0x140, ROR4 = 0x124, ROR8 = 0x128, BCAST = 0x150;
DEV v2f swap16(float a, float b) { auto s = __builtin_amdgcn_permlane16_swap(asu(a), asu(b), false, false); return v2f{asf(s[0]), asf(s[1])}; }
DEV v2f swap32(float a, float b) { auto s = __builtin_amdgcn_permlane32_swap(asu(a), asu(b), false, false); return v2f{asf(s[0]), asf(s[1])}; }
DEV float add16(float a, float b) { const v2f s = swap16(a, b); return s.x + s.y; }
DEV float add32(float a, float b) { const v2f s = swap32(a, b); return s.x + s.y; }
DEV float mul32(float a, float b) { const v2f s = swap32(a, b); return s.x * s.y; }
DEV float row_sum(float x) { x += dpp<X1>(x); x += dpp<X2>(x); x += dpp<X7>(x); x += dpp<X15>(x); return x; }
DEV float wave_sum(float x) { x = row_sum(x); x = add16(x, x); return add32(x, x); }
DEV u32 safe(u32 x) { return min(x, EMPTY - 1u); }
DEV u32 umax4(v4u v) { return max(max(v.x, v.y), max(v.z, v.w)); }
DEV v4u onehot(int t, u32 v) { return v4u{t == 0 ? v : 0u, t == 1 ? v : 0u, t == 2 ? v : 0u, t == 3 ? v : 0u}; }
DEV v4u lr16(LAS char* p) { return *(LAS v4u*)p; }
DEV u32 ldsr(LAS char* p) { return __builtin_amdgcn_readfirstlane(*(volatile LAS u32*)p); }
DEV v4u trfrag(LAS char* p0, LAS char* p1) {
  const v2u a = __builtin_bit_cast(v2u, __builtin_amdgcn_ds_read_tr16_b64_v4i16((LAS v4s*)p0));
  const v2u b = __builtin_bit_cast(v2u, __builtin_amdgcn_ds_read_tr16_b64_v4i16((LAS v4s*)p1));
  return v4u{a.x, a.y, b.x, b.y};
}


DEV u64 epoch_tag(rsrc_t rws) {
  const v2u e = ld8<AUX_SC1>(rws, WS_EPOCH, 0);
  return (((u64)(u32)__builtin_amdgcn_readfirstlane(e.y) << 32) | (u32)__builtin_amdgcn_readfirstlane(e.x)) + 1;
}
DEV bool await_flags(rsrc_t rws, u64 tag, u32 t0, int lane, u32& base) {
  v2u f; bool ok;
#pragma nounroll
  for (;;) {
    f = ld8<AUX_SC1>(rws, lane * 8, WS_FLAG);
    ok = __all(((f.y ^ (u32)(tag >> 8)) | ((f.x ^ (u32)tag) & 0xffu)) == 0);
    if (ok || expired(t0)) break;
    __builtin_amdgcn_s_sleep(64); FENCE();
  }
  FENCE();
  const float d = fminf(fmaxf((float)((int)((f.x & ~0xffu) - (t0 << 8)) >> 8), -1000.f), 1000.f);
  base = t0 + (u32)__builtin_amdgcn_readfirstlane((int)(wave_sum((lane & 7) == 7 ? 0.f : d) * (1.f / 56.f)));
  return ok;
}
DEV int round_shift(u32 raw) {
  const u32 v = raw + 1u, h = v + dppu<X1>(v);
  return (int)min((u32)__builtin_amdgcn_readfirstlane(h + dppu<X2>(h)), 400u * 64u);
}

struct Pacer {
  u32 tb; int lim, idx, late;
  DEV void operator()() {
    int e = late = fine(tb) - lim;
    while (e < 0) { __builtin_amdgcn_s_sleep(1); e = fine(tb) - lim; }
    lim += ++idx <= N1 ? TAU1 : TAU;
  }
};

struct Scorer {
  v4u q; LAS char* dst; bool hi8; float mx;
  DEV Scorer(LAS char* L, int w, int lane) {
    q = lr16(L + L_Q + (lane & 31) * 16);
    dst = (lane & 7) == 0 ? L + L_SC + w * SCW + (lane >> 5) * 16 + (((lane >> 4) & 1) + ((lane >> 2) & 2)) * 4 : L + L_SJ + lane * 4;
    hi8 = lane & 8; mx = -INFINITY;
  }
  DEV void operator()(v4u k0, v4u k1, v4u k2, v4u k3, int t, bool live) {
    float d0 = 0.f, d1 = 0.f, d2 = 0.f, d3 = 0.f;
#pragma unroll
    for (int i = 0; i < 4; ++i) { d0 = dot2(k0[i], q[i], d0); d1 = dot2(k1[i], q[i], d1); d2 = dot2(k2[i], q[i], d2); d3 = dot2(k3[i], q[i], d3); }
    const float e0 = add16(d0, d1), e1 = add16(d2, d3);
    const float f0 = e0 + dpp<ROR8>(e0), f1 = e1 + dpp<ROR8>(e1);
    float f = hi8 ? f1 : f0;
    f += dpp<X7>(f); f += dpp<X2>(f); f += dpp<X1>(f);
    f = live ? rbf(f) * (0.0625f * 1.4426950408889634f) : -INFINITY;
    *(LAS float*)(dst + t * 32) = f;
    mx = fmaxf(mx, f);
  }
  DEV float wave_max() {
    const float a = fmaxf(mx, dpp<ROR8>(mx));
    const v2f b = swap16(a, a); const float c = fmaxf(b.x, b.y);
    const v2f d = swap32(c, c); return fmaxf(d.x, d.y);
  }
};

DEV void await_q(LAS char* L) {
  while (ldsr(L + L_QOK) == 0u) __builtin_amdgcn_s_sleep(2);
  FENCE();
}

DEV void steady_wave(const Args& a, LAS char* L, int bh, int s, int w, int lane, int phase, u32 t0) {
  const rsrc_t rk = mkrs(a.K + ((size_t)bh << 22), 1u << 22), rv = mkrs(a.V + ((size_t)bh << 22), 1u << 22);
  const int np = s < 4 ? NP : NP - 1;
  const u32 voff = lane * 16 + w * 4096 + s * 16384;
  Pacer pace; pace.tb = t0 + phase / 64; pace.lim = sched(NE); pace.idx = NE;
  bool flags_ok = true;
  if (w == 0) {
    const rsrc_t rws = mkrs(a.ws, WS_END);
    const u64 tag = epoch_tag(rws);
    wait_until(pace.tb + FLAG_T);
    u32 tg;
    flags_ok = await_flags(rws, tag, t0, lane, tg);
    *(LAS u32*)(L + L_TG) = tg;
  }
  const int g = lane >> 4, n = lane & 15;
  v4u msk = onehot((n & 7) >> 1, (n >> 3) == (g & 1) ? ((n & 1) ? 0xffff0000u : 0x0000ffffu) : 0u);
  LAS char* pr = L + L_SC + w * SCW + (lane >> 5) * 16;
  LAS char* accw = L + L_ACC + w * 1024 + ((lane >> 5) * 16 + n) * 32 + (g & 1) * 16;
  asm volatile("" : "+v"(accw), "+v"(pr), "+v"(msk));
  __builtin_amdgcn_s_setprio(2);

  const int sq = phase / 3;
  int applied = 0, target = 0, gap = 0;
  auto follow = [&](int d) {
    if (d == 0) target = (int)ldsr(L + L_CD);
    if (w == 1) *(LAS int*)(L + L_CE) = pace.late + applied;
    const int inc = (target - applied + 3) >> 2, gs = min(max(gap, -SLEW), SLEW);
    applied += inc; gap -= gs; pace.lim += inc + gs;
  };
  v4u buf[3][4];
  auto issue = [&](int d, rsrc_t rs, u32 vo, u32 so) {
#pragma unroll
    for (int i = 0; i < 4; ++i) { buf[d][i] = ld16<AUX_NT>(rs, vo + i * 1024, so); FENCE(); }
  };

  const rsrc_t rq = mkrs(a.ws + WS_Q + bh * 512, w == 0 ? 512 : 0);
  wait_until(pace.tb + Q_T);
  FENCE();
  const v2u q0 = ld8<AUX_SC1>(rq, lane * 8, 0);
  FENCE();
  pace(); issue(0, rk, voff, NE * PSTR);
  pace(); issue(1, rk, voff, (NE + 1) * PSTR);
  if (w == 0) {
    *(LAS v2u*)(L + L_Q + lane * 8) = flags_ok ? q0 : v2u{EMPTY, EMPTY};
    if (flags_ok && !__all(max(q0.x, q0.y) != EMPTY)) {
#pragma nounroll
      for (;;) {
        FENCE();
        const v2u q1 = ld8<AUX_SC1>(rq, lane * 8, 0);
        *(LAS v2u*)(L + L_Q + lane * 8) = q1;
        if (__all(max(q1.x, q1.y) != EMPTY) || expired(t0)) break;
      }
    }
    FENCE();
    *(volatile LAS u32*)(L + L_QOK) = 1u;
  }
  await_q(L);
  gap = (int)(ldsr(L + L_TG) - t0) * 64;

  Scorer score(L, w, lane);
#pragma nounroll
  for (int t = NE; t < NP; t += 3) {
#pragma unroll
    for (int d = 0; d < 3; ++d) {
      const int u = t + d + 2;
      const bool isv = d > 0 && u >= NP;
      if (d < 2 && t == NP - 3) pace.lim += d == 0 ? -sq : sq - TAU / 3;
      pace();
      issue((d + 2) % 3, isv ? rv : rk, (d == 0 && u >= np) ? OOB : voff, (isv ? u - NP : u) * PSTR);
      follow(d);
      score(buf[d][0], buf[d][1], buf[d][2], buf[d][3], t + d, d < 2 || t + d < np);
      FENCE();
    }
  }

  float m = 0.f;
  v4f acc = {0.f, 0.f, 0.f, 0.f};
  auto pv = [&](int d, int t) {
    const v4f sv = *(LAS v4f*)(pr + t * 32);
    v4u pa[4];
#pragma unroll
    for (int i = 0; i < 4; ++i) { const float e = __builtin_amdgcn_exp2f(sv[i] - m); pa[i] = msk & pk(e, e); }
    FENCE();
    acc = mfma(pa[3], buf[d][3], mfma(pa[2], buf[d][2], mfma(pa[1], buf[d][1], mfma(pa[0], buf[d][0], acc))));
    asm volatile("" : "+v"(acc));
    FENCE();
  };
#pragma nounroll
  for (int t = 0; t < NP - 1; t += 3) {
#pragma unroll
    for (int d = 0; d < 3; ++d) {
      const int u = t + d + 2;
      if (d == 1 && u == NP - 1) pace.lim -= sq;
      if (d == 0 || u < np) pace();
      issue((d + 2) % 3, rv, (d == 0 || u < np) ? voff : OOB, u * PSTR);
      if (d == 0 && t == 0) {
        v2u em = *(volatile LAS v2u*)(L + L_EM + w * 8);
        m = score.wave_max();
        while (__builtin_amdgcn_readfirstlane(em.y) == 0u) em = *(volatile LAS v2u*)(L + L_EM + w * 8);
        m = fmaxf(m, asf(em.x));
        *(volatile LAS v2u*)(L + L_ML + w * 8) = v2u{asu(m), 1u};
        FENCE();
      }
      follow(d);
      pv(d, t + d);
    }
  }
  pv(0, NP - 1);
  *(LAS v4f*)accw = acc;
  bar();
  asm volatile("" :: "v"(buf[1][0]), "v"(buf[1][1]), "v"(buf[1][2]), "v"(buf[1][3]));
}

DEV void early_wave(const Args& a, LAS char* L, int bh, int s, int w, int lane, Pacer pace, int slot, u32 t0) {
  const rsrc_t rws = mkrs(a.ws, WS_END);
  u32 tg = 0; int ev = 0, shift = 0;
  auto tick = [&]() {
    const int k = ev >> 2, st = ev & 3;
    const u32 tk = C0 + k * CPER, dt = now() - tg;
    if (k >= CN || dt < tk + (st == 0 ? 0 : st == 1 ? CRD : CRD + 70)) return;
    if (st == 0) {
      const int id = blockIdx.x - 64, rid = id / 3, e = ((int)ldsr(L + L_CE) - shift) >> 6;
      if (id == rid * 3 && e > CMIN && dt < tk + CWIN)
        __builtin_amdgcn_raw_ptr_buffer_atomic_add_i32(min(e + CADD, 400), rws, lane == 0 ? (rid & 3) * 128 : OOB, WS_CTL + k * 512, 0);
    } else if (st == 1) {
      DMA("buffer_load_dword", "sc1", rws, laddr(L + L_CR), lane < 4 ? lane * 128 : OOB, WS_CTL + k * 512);
    } else {
      VMWAIT(0);
      shift += round_shift(*(LAS u32*)(L + L_CR + (lane & 3) * 4));
      *(LAS int*)(L + L_CD) = shift;
      ++ev;
    }
    ++ev;
  };
  {
    const rsrc_t rk = mkrs(a.K + ((size_t)bh << 22), 1u << 22);
    const u32 voff = lane * 16 + w * 4096 + s * 16384;
    v4u buf[NR][4];
#pragma unroll
    for (int t = 0; t < NR; ++t) {
      pace();
#pragma unroll
      for (int i = 0; i < 4; ++i) { asm volatile(WINDOW ::: "memory"); buf[t][i] = ld16<AUX_NT>(rk, voff + i * 1024, (NL + t) * PSTR); FENCE(); }
    }
    VMWAIT(0);
    await_q(L);
    tg = ldsr(L + L_TG);
    Scorer score(L, w, lane);
    LAS char* kp = L + L_KB + w * 4096 + lane * 16;
#pragma nounroll
    for (int t = 0; t < NE; ++t) {
      if (t == NL) {
#pragma unroll
        for (int u = 0; u < NR * 4; ++u) *(LAS v4u*)(kp + (u >> 2) * 16384 + (u & 3) * 1024) = buf[u >> 2][u & 3];
      }
      LAS char* p = kp + (t < NL ? t : t - NL) * 16384;
      score(lr16(p), lr16(p + 1024), lr16(p + 2048), lr16(p + 3072), t, true);
      FENCE();
      if (w == 1) tick();
    }
    const float em = score.wave_max();
    FENCE();
    *(volatile LAS v2u*)(L + L_EM + w * 8) = v2u{asu(em), 1u};
  }
  if (w == 1) {
#pragma nounroll
    while (ev < 4 * CN) { tick(); __builtin_amdgcn_s_sleep(2); }
  }
  if (w != 0) return;

  v2u mw = *(volatile LAS v2u*)(L + L_ML + (lane & 3) * 8);
  while (!__all(mw.y != 0u)) { __builtin_amdgcn_s_sleep(16); mw = *(volatile LAS v2u*)(L + L_ML + (lane & 3) * 8); }
  FENCE();
  const float mi = asf(mw.x), m2 = fmaxf(mi, dpp<X1>(mi)), M = fmaxf(m2, dpp<X2>(m2));
  float l = 0.f;
#pragma nounroll
  for (int i = 0; i < 4; ++i) {
    const bool two = lane < 2 * NP - 64;
    const v4f sa = *(LAS v4f*)(L + L_SC + i * SCW + lane * 16), sb = *(LAS v4f*)(L + L_SC + i * SCW + 1024 + (two ? lane : 0) * 16);
    float l2 = 0.f;
#pragma unroll
    for (int k = 0; k < 4; ++k) { l += __builtin_amdgcn_exp2f(sa[k] - M); l2 += __builtin_amdgcn_exp2f(sb[k] - M); }
    if (two) l += l2;
  }
  l = wave_sum(l);
  const float f = __builtin_amdgcn_exp2f(mi - M) / l;
  float fac[4];
#pragma unroll
  for (int i = 0; i < 4; ++i) fac[i] = asf(__builtin_amdgcn_readlane(asu(f), i));
  const int rec = (bh & 15) * 12 + (bh >> 4) * 6 + s;
  wait_until(tg + C0 + CN * CPER);
  wait_until(tg + (ldsr(L + L_CD) >> 6) + LAST - (31 - slot) * (TAU / 48) / 64 + 4);
  put8(v2u{safe(asu(M)), safe(asu(l))}, rws, lane == 0 ? 0u : OOB, WS_ML + rec * 128);

  u32 oso = WS_O + rec * 512, ovo = lane * 8;
  LAS char* ar = L + L_ACC + lane * 16;
  asm volatile("" : "+s"(fac[0]), "+s"(fac[1]), "+s"(fac[2]), "+s"(fac[3]), "+s"(oso));
  asm volatile("" : "+v"(ovo), "+v"(ar));
  __builtin_amdgcn_s_setprio(3);
  bar();
  const v4f a0 = *(LAS v4f*)ar, a1 = *(LAS v4f*)(ar + 1024), a2 = *(LAS v4f*)(ar + 2048), a3 = *(LAS v4f*)(ar + 3072);
  FENCE();
  const v4f o = (a0 * fac[0] + a1 * fac[1]) + (a2 * fac[2] + a3 * fac[3]);
  put8(v2u{safe(pk(o.x, o.y)), safe(pk(o.z, o.w))}, rws, ovo, oso);
}

DEV void proj_wave(const Args& a, LAS char* L, int x, int j, int w, int lane, u32 t0, u64 tag) {
  const int g = lane >> 4, n = lane & 15;
  const rsrc_t rws = mkrs(a.ws, WS_END);
  const v4f zf = {0.f, 0.f, 0.f, 0.f};
  LAS char* img = L + L_WO + w * 16384;
  v4u wf[4][4];
#pragma unroll
  for (int i = 0; i < 16; ++i) { LAS char* p = img + ((i >> 2) * 32 + g * 8 + (n >> 2)) * 128 + (i & 3) * 32 + (n & 3) * 8; wf[i >> 2][i & 3] = trfrag(p, p + 512); }
  const int prow = (n >> 3) * 6 + (n & 7), ms = (n & 7) ^ ((g & 1) * 4);
  u32 ovo[3]; LAS char* td[4];
#pragma unroll
  for (int k = 0; k < 3; ++k) ovo[k] = (k * 4 + g) * 512 + (n ^ (k * 4 + g)) * 16;
#pragma unroll
  for (int t = 0; t < 4; ++t) td[t] = (n & 7) < 6 ? img + prow * 256 + ((t * 4 + g) ^ prow) * 16 : L + L_ZERO;
  LAS char* yw = L + L_YR + w * 512 + (lane & 31) * 8 + (lane >> 5) * 4;
  LAS char* yr = L + L_YR + lane * 8;
  LAS char* mr = img + 4096 + lane * 16;
  u32 yvo = lane * 4, mvo = ms < 6 && n < 8 ? ((g >> 1) * 6 + ms) * 128 : OOB;
  float mdead = ms < 6 ? 0.f : -INFINITY;
  asm volatile("s_waitcnt lgkmcnt(0)" : "+v"(ovo[0]), "+v"(ovo[1]), "+v"(ovo[2]), "+v"(td[0]), "+v"(td[1]), "+v"(td[2]), "+v"(td[3]),
               "+v"(yw), "+v"(yr), "+v"(mr), "+v"(yvo), "+v"(mvo), "+v"(mdead) :: "memory");
  const int head = x * 2 + (w >> 1);
  u32 oso = WS_O + head * 6144 + (w & 1) * 256, mso = WS_ML + head * 1536, yso = WS_Y + w * YSTR + x * 1792 + j * 256, dst = laddr(img);
  asm volatile("" : "+s"(oso), "+s"(mso), "+s"(yso), "+s"(dst));
  auto fetch_ml = [&]() { DMA("buffer_load_dwordx4", "sc1", rws, dst + 4096, mvo, mso); };
  auto fetch_o = [&]() {
    u32 keep;
    asm volatile("s_mov_b32 %0, m0\n\ts_mov_b32 m0, %1\n\ts_nop 0\n\tbuffer_load_dwordx4 %2, %5, %6 offen sc1 lds\n\t"
                 "s_add_u32 m0, m0, 0x400\n\ts_nop 0\n\tbuffer_load_dwordx4 %3, %5, %6 offen sc1 lds\n\t"
                 "s_add_u32 m0, m0, 0x400\n\ts_nop 0\n\tbuffer_load_dwordx4 %4, %5, %6 offen sc1 lds\n\ts_mov_b32 m0, %0"
                 : "=&s"(keep) : "s"(dst), "v"(ovo[0]), "v"(ovo[1]), "v"(ovo[2]), "s"(rws), "s"(oso) : "memory", "scc");
  };
  float wv[4]; u32 mlx;
  auto weights = [&]() {
    VMWAIT(3);
    const v2u ml = *(LAS v2u*)mr;
    const float m = asf(ml.x) + mdead;
    float M = fmaxf(m, dpp<X1>(m)); M = fmaxf(M, dpp<X2>(M)); M = fmaxf(M, dpp<X7>(M));
    const float e = __builtin_amdgcn_exp2f(m - M) * asf(ml.y);
    float sum = e + dpp<X1>(e); sum += dpp<X2>(sum); sum += dpp<X7>(sum);
    const float wt = e * __builtin_amdgcn_rcpf(sum);
    wv[0] = dpp<BCAST>(wt); wv[1] = dpp<BCAST + 1>(wt); wv[2] = dpp<BCAST + 2>(wt); wv[3] = dpp<BCAST + 3>(wt);
    mlx = max(ml.x, ml.y);
    asm volatile("s_waitcnt vmcnt(0)" : "+v"(wv[0]), "+v"(wv[1]), "+v"(wv[2]), "+v"(wv[3]), "+v"(mlx) :: "memory");
  };
  const u32 tb = ldsr(L + L_TB);
  wait_until(tb + ML_T);
  fetch_ml();
  spin_until(tb + O_T);
  fetch_o();
  weights();
  bool give_up = false;
#pragma nounroll
  for (;;) {
    FENCE();
    v4u af[4];
#pragma unroll
    for (int t = 0; t < 4; ++t) af[t] = lr16(td[t]);
    FENCE();
    auto tile = [&](int c) { return mfma(af[3], wf[3][c], mfma(af[2], wf[2][c], mfma(af[1], wf[1][c], mfma(af[0], wf[0][c], zf)))); };
    auto wsum = [&](v4f v) { return fmaf(v.w, wv[3], fmaf(v.z, wv[2], fmaf(v.y, wv[1], v.x * wv[0]))); };
    v4f y0 = tile(0), y1 = tile(1);
    const u32 mx = max(max(max(umax4(af[0]), umax4(af[1])), max(umax4(af[2]), umax4(af[3]))), mlx);
    INTERLEAVE(8, 2)
    asm volatile("" : "+v"(y0), "+v"(y1));
    if (__builtin_expect(__all(mx != EMPTY) || give_up, 1)) {
      FENCE();
      v4f y2 = tile(2); const float s0 = wsum(y0);
      INTERLEAVE(4, 1)
      asm volatile("" : "+v"(y2));
      FENCE();
      v4f y3 = tile(3); const float e0 = add16(s0, wsum(y1));
      INTERLEAVE(4, 2)
      asm volatile("" : "+v"(y3));
      FENCE();
      *(LAS float*)yw = e0;
      const float s2 = wsum(y2);
      FENCE();
      *(LAS float*)(yw + 256) = add16(s2, wsum(y3));
      break;
    }
    FENCE();
    fetch_ml(); fetch_o();
    give_up = expired(t0);
    weights();
  }
  bar();
  const v2f u = (*(LAS v2f*)yr + *(LAS v2f*)(yr + 512)) + (*(LAS v2f*)(yr + 1024) + *(LAS v2f*)(yr + 1536));
  put4(safe(pk(u.x, u.y)), rws, yvo, yso);
  if (w == 0) {
    LAS char* sr = lane < 56 ? L + L_HB + lane * 16 : L + L_ZERO;
    const int step = lane < 56 ? D * 2 : 0;
    bar();
    const v4u h0 = lr16(sr), h1 = lr16(sr + step);
    float s0 = 0.f, s1 = 0.f, q0 = 0.f, q1 = 0.f;
#pragma unroll
    for (int i = 0; i < 4; ++i) { s0 = dot2(h0[i], ONES, s0); s1 = dot2(h1[i], ONES, s1); q0 = dot2(h0[i], h0[i], q0); q1 = dot2(h1[i], h1[i], q1); }
    const float st = row_sum(add16(add32(s0, s1), add32(q0, q1))) * (1.f / D);
    const float var = add16((g & 1) ? st : -st * st, 0.f);
    if ((lane & 31) == 0) *(LAS float*)(L + L_R2 + (lane >> 5) * 4) = __builtin_amdgcn_rcpf(fmaxf(var, 0.f) + 1e-5f);
    bar();
    put8(v2u{(u32)tag, (u32)(tag >> 32)}, rws, (lane == 0 && x == 0 && j == 0) ? WS_EPOCH : OOB, 0);
  }
}

DEV void ffn_wave(const Args& a, LAS char* L, int x, int j, int w, int lane, u32 t0) {
  const int g = lane >> 4, n = lane & 15, fj = x * 7 + j;
  const rsrc_t rws = mkrs(a.ws, WS_END), rd = mkrs(a.wd, D * F * 2), ro = mkrs(a.out, 2 * D * 2);
  const v4f zf = {0.f, 0.f, 0.f, 0.f};
  const v4u z4 = {0u, 0u, 0u, 0u}, o4 = {ONES, ONES, ONES, ONES};
  v4u guf[14], wdf[7];
  v4f cs = zf;
#pragma unroll
  for (int t = 0; t < 14; ++t) {
    LAS char* p = L + L_WG + ((n >> 1) & 1) * 28672 + (t * 32 + g * 8 + (n >> 2)) * 64 + w * 16 + (n & 1) * 8;
    guf[t] = trfrag(p, p + 256);
    cs = mfma(guf[t], o4, cs);
  }
  cs *= -1.f / D;
  const int pc = w * 32 + (g >> 1) * 16 + n, hv = pc < 112 ? (g & 1) * D + pc * 4 : 2 * D;
  const v2u xx = ld8<0>(mkrs(a.x, 2 * D * 2), hv * 2, 0);
  v4f xc = {lo16(xx.x), hi16(xx.x), lo16(xx.y), hi16(xx.y)};
  const v4u dg = onehot(n & 3, (n & 4) ? 0x3F800000u : 0x00003F80u);
  v4u sel0 = n < 8 ? dg : z4, sel1 = n < 8 ? z4 : dg, on0 = (n & 1) ? z4 : o4, on1 = (n & 1) ? o4 : z4;
  const int tA = w * 7 + g, tB = w * 7 + 4 + g;
  const bool rA = tA == fj, rB = tB == fj && g < 3;
  LAS char* hA = L + (rA ? L_HF + (tA * 16 + n) * 4 : L_ZERO);
  LAS char* hB = L + (rB ? L_HF + (tB * 16 + n) * 4 : L_ZERO);
  LAS char* hA1 = hA + (rA ? D * 4 : 0);
  LAS char* hB1 = hB + (rB ? D * 4 : 0);
  LAS char* ga[4];
#pragma unroll
  for (int v = 0; v < 4; ++v) ga[v] = L + ((n >> 2) == v && (n & 2) == 0 ? L_GU + (n & 1) * 64 + g * 16 : L_ZERO);
  LAS char* hbw = L + (pc < 112 ? L_HB + hv * 2 : L_JUNK);
  LAS char* hfw = L + (pc < 112 ? L_HF + hv * 4 : L_JUNK);
  LAS char* hr = L + L_HB + (n & 1) * (D * 2) + g * 16;
  LAS char* s0r = lane < 56 ? L + L_HB + lane * 16 : L + L_ZERO;
  LAS char* s1r = lane < 56 ? L + L_HB + D * 2 + lane * 16 : L + L_ZERO;
  LAS char* gw = L + (n < 2 ? L_GU + n * 64 + w * 16 + (g & 1) * 8 + (g >> 1) * 4 : L_JUNK + lane * 4);
  u32 yo0 = g * 1792 + (w * 32 + n) * 16, yo1 = w < 3 ? yo0 + 256 : OOB;
  u32 fo0 = tA * FSTR + n * 8, fo1 = g < 3 ? tB * FSTR + n * 8 : OOB;
  u32 fr = (lane >> 2) * 128 + (lane & 3) * 16, fr3 = lane < 32 ? fr + 2048 : OOB;
  u32 oo = n < 4 ? (g >> 1) * (D * 2) + (n * 2 + (g & 1)) * 2 : OOB;
  u32 ys = WS_Y + (x & 3) * YSTR, ys1 = ys + 4 * 1792, fs = WS_F + fj * 128;
  u32 gs = WS_F + (fj >> 1) * FSTR + (fj & 1) * 64, gs1 = gs + 2048, gs2 = gs + 4096, os = fj * 16;
  asm volatile("" : "+s"(ys), "+s"(ys1), "+s"(fs), "+s"(gs), "+s"(gs1), "+s"(gs2), "+s"(os));
  asm volatile("" : "+v"(cs), "+v"(xc), "+v"(sel0), "+v"(sel1), "+v"(on0), "+v"(on1), "+v"(yo0), "+v"(yo1), "+v"(fo0), "+v"(fo1), "+v"(fr), "+v"(fr3), "+v"(oo));
  asm volatile("" : "+v"(hA), "+v"(hB), "+v"(hA1), "+v"(hB1), "+v"(ga[0]), "+v"(ga[1]), "+v"(ga[2]), "+v"(ga[3]), "+v"(hbw), "+v"(hfw), "+v"(hr), "+v"(s0r), "+v"(s1r), "+v"(gw));

  if (w == 1) __builtin_amdgcn_raw_buffer_store_b16((unsigned short)0, ro, oo, os, 0);
  if (w == 2) DMA("buffer_load_dword", "nt", rd, laddr(L + L_JUNK), lane < 2 ? lane * (32 * D * 2 - 4) : OOB, fj * (32 * D * 2));
  SETTLE(1);
  wait_until(ldsr(L + L_TB) + WD_T);
#pragma unroll
  for (int i = 0; i < 7; ++i) DMA("buffer_load_dwordx4", "", rd, laddr(L + L_WD) + w * 1024 + i * 4096, lane * 16 + w * 1024 + fj * (32 * D * 2), i * 4096);
  VMWAIT(0);
  bar();

  spin_until(now() + Y_D);
  auto load_y = [&](v4u (&d)[4]) {
    d[0] = ld16<AUX_SC1>(rws, yo0, ys); d[1] = ld16<AUX_SC1>(rws, yo0, ys1); d[2] = ld16<AUX_SC1>(rws, yo1, ys); d[3] = ld16<AUX_SC1>(rws, yo1, ys1);
    FENCE();
  };
  auto make_h = [&](v4u (&d)[4]) {
    const v4f h = mfma(sel1, d[3], mfma(sel1, d[2], mfma(sel0, d[1], mfma(sel0, d[0], xc))));
    const u32 mx = max(max(umax4(d[0]), umax4(d[1])), max(umax4(d[2]), umax4(d[3])));
    INTERLEAVE(4, 2)
    *(LAS v2u*)hbw = v2u{pk(h.x, h.y), pk(h.z, h.w)};
    *(LAS v4f*)hfw = h;
    return __all(mx != EMPTY);
  };
  v4u yl[4];
  load_y(yl);
#pragma unroll
  for (int t = 0; t < 7; ++t) { LAS char* p = L + L_WD + (g * 8 + (n >> 2)) * (D * 2) + (w * 7 + t) * 32 + (n & 3) * 8; wdf[t] = trfrag(p, p + 4 * D * 2); }
  asm volatile("s_waitcnt lgkmcnt(0)" ::: "memory");
  FENCE();
  if (__builtin_expect(!make_h(yl), 0)) {
#pragma nounroll
    for (;;) {
      FENCE();
      v4u yb[4];
      load_y(yb);
      if (make_h(yb) || expired(t0)) break;
    }
  }
  bar();

  v4u hf[14];
#pragma unroll
  for (int t = 0; t < 3; ++t) hf[t] = lr16(hr + t * 64);
  const v4u h0 = lr16(s0r), h1 = lr16(s1r);
#pragma unroll
  for (int t = 3; t < 10; ++t) hf[t] = lr16(hr + t * 64);
  FENCE();
  v4f gu = mfma(guf[2], hf[2], mfma(guf[1], hf[1], mfma(guf[0], hf[0], zf)));
  FENCE();
#pragma unroll
  for (int t = 10; t < 14; ++t) hf[t] = lr16(hr + t * 64);
  const v4f ms = mfma(h1, on1, mfma(h0, on0, zf));
  FENCE();
#pragma unroll
  for (int t = 3; t < 14; ++t) gu = mfma(guf[t], hf[t], gu);
  float sm = (ms.x + ms.y) + (ms.z + ms.w);
  sm = add16(sm, sm); sm = add32(sm, sm);
  INTERLEAVE(11, 1)
  const v4f c = gu + sm * cs;
  *(LAS u32*)gw = pk(mul32(c.x, c.z), mul32(c.y, c.w));
  bar();

  const v4u g0 = lr16(ga[0]), g1 = lr16(ga[1]), g2 = lr16(ga[2]), g3 = lr16(ga[3]);
  const v2f r2 = *(LAS v2f*)(L + L_R2);
  const v2f resA = {*(LAS float*)hA, *(LAS float*)hA1}, resB = {*(LAS float*)hB, *(LAS float*)hB1};
  FENCE();
  const v4f dA = mfma(g3, wdf[3], mfma(g2, wdf[2], mfma(g1, wdf[1], mfma(g0, wdf[0], zf))));
  FENCE();
  const v4f dB = mfma(g2, wdf[6], mfma(g1, wdf[5], mfma(g0, wdf[4], zf)));
  FENCE();
  const v2f oA = v2f{dA.x, dA.y} * r2 + resA;
  put8(v2u{safe(asu(oA.x)), safe(asu(oA.y))}, rws, fo0, fs);
  FENCE();
  const v2f oB = v2f{dB.x, dB.y} * r2 + resB;
  put8(v2u{safe(asu(oB.x)), safe(asu(oB.y))}, rws, fo1, fs);

  if (w == 1) {
    spin_until(now() + F_D);
    float o;
#pragma nounroll
    for (;;) {
      const v4u f0 = ld16<AUX_SC1>(rws, fr, gs), f1 = ld16<AUX_SC1>(rws, fr, gs1), f2 = ld16<AUX_SC1>(rws, fr, gs2), f3 = ld16<AUX_SC1>(rws, fr3, gs2);
      FENCE();
      const v4f t = ((__builtin_bit_cast(v4f, f0) + __builtin_bit_cast(v4f, f1)) + __builtin_bit_cast(v4f, f2)) + __builtin_bit_cast(v4f, f3);
      o = add16(add32(t.x, t.y), add32(t.z, t.w));
      o += dpp<ROR4>(o); o += dpp<ROR8>(o);
      asm volatile("" : "+v"(o));
      if (__all(o == o) || __all(max(max(umax4(f0), umax4(f1)), max(umax4(f2), umax4(f3))) != EMPTY) || expired(t0)) break;
      FENCE();
    }
    __builtin_amdgcn_raw_buffer_store_b16((unsigned short)pk(o, o), ro, oo, os, 0);
  }
}

DEV bool service_loader(const Args& a, LAS char* L, int x, int j, int w, int lane, u32 t0, rsrc_t rcode) {
  int due = PULL0 + w * (WSTEP / 4);
  auto run = [&](rsrc_t rs, u32 vo, u32 sstep, u32 dst, u32 dstep, int lo, int hi) {
#pragma nounroll
    for (int i = lo; i < hi; ++i) {
      while (fine(t0) < due) __builtin_amdgcn_s_sleep(1);
      DMA("buffer_load_dwordx4", "", rs, dst + i * dstep, vo, i * sstep);
      due += WSTEP;
    }
  };
  run(mkrs(a.wq, D * HD * 2), (w * 8 + (lane >> 3)) * (HD * 2) + ((lane & 7) ^ ((lane >> 3) & 2) ^ ((w & 1) << 2)) * 16 + (x * 8 + j) * 128,
      32 * HD * 2, laddr(L + L_WQ) + w * 1024, 4096, 0, 14);
  VMWAIT(0);
  bar();
  if (j == 7) {
    if (w < 2) {
      wait_until(t0 + (w ? 1200 : 640));
      DMA("buffer_load_dword", "nt", mkrs(w ? a.V : a.K, 1u << 27), laddr(L + L_JUNK), lane < 8 ? (lane << 21) + 2048 : OOB, x << 24);
    } else if (w == 2) {
#pragma nounroll
      for (int k = 0; k < 9; ++k) {
        wait_until(t0 + (k < 3 ? 600 : k < 6 ? 1700 : 3300));
        DMA("buffer_load_dword", "sc1", rcode, laddr(L + L_JUNK), lane * 128 + (k % 3) * 8192, 0);
      }
    }
    return false;
  }
  run(mkrs((w & 1) ? a.wu : a.wg, D * F * 2), ((w >> 1) * 16 + (lane >> 2)) * (F * 2) + (lane & 3) * 16 + (x * 7 + j) * 64,
      32 * F * 2, laddr(L + L_WG) + (w & 1) * 28672 + (w >> 1) * 1024, 2048, 0, 14);
  bar();
  run(mkrs(a.wo, HD * D * 2), (x * 512 + w * 8 + (lane >> 3)) * (D * 2) + (lane & 7) * 16 + j * 128, 32 * D * 2, laddr(L + L_WO) + w * 1024, 4096, 0, 16);
  VMWAIT(0);
  bar();
  return true;
}

DEV bool service_front(const Args& a, LAS char* L, int x, int j, int w, int lane, u32 t0, u64& tag) {
  const int g = lane >> 4, n = lane & 15, id = x * 8 + j;
  const rsrc_t rws = mkrs(a.ws, WS_END);
  if (w == 3) {
    const v4u e4 = {EMPTY, EMPTY, EMPTY, EMPTY};
    const u32 co = WS_ML + id * CLR;
#pragma unroll
    for (int i = 0; i < 6; ++i) put16(e4, rws, lane * 16 + (i & 3) * 1024, co + (i >> 2) * 4096);
    put16(e4, rws, lane < 16 ? co + 6144 + lane * 16 : lane < 32 ? WS_Q + id * 128 + ((lane >> 3) & 1) * (HD * 2) + (lane & 7) * 16 : OOB, 0);
    tag = epoch_tag(rws);
    VMWAIT(0);
    put8(v2u{(t0 << 8) | ((u32)tag & 0xffu), (u32)(tag >> 8)}, rws, lane == 0 ? WS_FLAG + id * 8 : OOB, 0);
  } else if (w == 2) {
    if (lane < 4) *(LAS u32*)(L + L_ZERO + lane * 4) = 0u;
  } else {
    const v4u xr = ld16<0>(mkrs(a.x, 2 * D * 2), lane < 56 ? lane * 16 : OOB, w * (D * 2));
    tag = epoch_tag(rws);
    float v[8] = {lo16(xr.x), hi16(xr.x), lo16(xr.y), hi16(xr.y), lo16(xr.z), hi16(xr.z), lo16(xr.w), hi16(xr.w)};
    const float mean = wave_sum(((v[0] + v[1]) + (v[2] + v[3])) + ((v[4] + v[5]) + (v[6] + v[7]))) / D;
    float ss = 0.f;
#pragma unroll
    for (int i = 0; i < 8; ++i) ss = fmaf(v[i] - mean, v[i] - mean, ss);
    const float sd = rbf(sqrtf(rbf(rbf(wave_sum(lane < 56 ? ss : 0.f) / D) + 1e-5f))), mb = rbf(mean);
#pragma unroll
    for (int i = 0; i < 8; ++i) v[i] = rbf(v[i] - mb) / sd;
    if (lane < 56) *(LAS v4u*)(L + L_XN + w * (D * 2) + lane * 16) = v4u{pk(v[0], v[1]), pk(v[2], v[3]), pk(v[4], v[5]), pk(v[6], v[7])};
  }
  bar();
  v4f qa = {0.f, 0.f, 0.f, 0.f};
#pragma unroll
  for (int t = 0; t < 14; ++t) {
    LAS char* p = L + L_WQ + (t * 32 + g * 8 + (n >> 2)) * 128 + (w ^ (n >> 3) ^ ((g & 1) << 1)) * 32 + (n & 3) * 8;
    qa = mfma(lr16(L + L_XN + (n & 1) * (D * 2) + t * 64 + g * 16), trfrag(p, p + 512), qa);
  }
  const float qv = (g & 1) ? qa.y : qa.x;
  put4(safe(pk(qv, dpp<X1>(qv))), rws, g < 2 && !(n & 1) ? g * (HD * 2) + n * 2 : OOB, WS_Q + id * 128 + w * 32);
  if (j == 7) return false;
  bar();
  if (w == 3) {
    u32 tg;
    if (!await_flags(rws, tag, t0, lane, tg)) *(LAS u32*)(L + L_ZERO) = EMPTY - 1u;
    int shift = 0;
#pragma nounroll
    for (int k = 0; k < CN; ++k) { wait_until(tg + C0 + k * CPER + CRD); FENCE(); shift += round_shift(ld4<AUX_SC1>(rws, (lane & 3) * 128, WS_CTL + k * 512)); }
    *(LAS u32*)(L + L_TB) = tg + (shift >> 6) + LAST;
  }
  bar();
  return true;
}

DEV char* mkptr(u32 lo, u32 hi) { return (char*)(((u64)hi << 32) | lo); }

__global__ void __launch_bounds__(512, 2) fused_decode(Args) {
  extern __shared__ __attribute__((aligned(16))) char smem[];
  LAS char* L = (LAS char*)smem;
  v16u ka; v4u kb; u64 pc; u32 code_bytes;
  asm volatile("s_load_dwordx16 %0, %4, 0x0\n\ts_load_dwordx4 %1, %4, 0x40\n\ts_getpc_b64 %2\n.Lfused_first:\n\ts_mov_b32 %3, .Lfused_last-.Lfused_first"
               : "=&s"(ka), "=&s"(kb), "=&s"(pc), "=&s"(code_bytes) : "s"(__builtin_amdgcn_kernarg_segment_ptr()));
  const rsrc_t rcode = mkrs((const char*)pc, code_bytes);
  const u32 junk = laddr(L + L_JUNK);
  if ((blockIdx.x >> 3) == 7) DMA("buffer_load_dword", "", rcode, junk, threadIdx.x * 64, 0);

  const int lane = threadIdx.x & 63, x = blockIdx.x & 7;
  int w = __builtin_amdgcn_readfirstlane(threadIdx.x >> 6), r = blockIdx.x >> 3;
  const int hw = w & 3, qd = r >= 8 ? ((r - 8) * 43) >> 8 : r & 3, s = r - 8 - qd * 6, bh = x * 4 + qd, slot = qd * 8 + x;
  u32 kvo = lane * 16 + hw * 4096 + s * 16384, one = lane == 0 ? 0u : OOB;
  u32 kdst = laddr(L + L_KB) + hw * 4096, koff = (u32)bh << 22;
  u64 ta;
  asm volatile("" : "+v"(kvo), "+v"(one));
  asm volatile("s_waitcnt lgkmcnt(0)\n\ts_memrealtime %4" : "+s"(ka), "+s"(kb), "+s"(kdst), "+s"(koff), "=&s"(ta));
  Args a;
  a.x = mkptr(ka[0], ka[1]); a.K = mkptr(ka[2], ka[3]); a.V = mkptr(ka[4], ka[5]); a.wq = mkptr(ka[6], ka[7]);
  a.wo = mkptr(ka[8], ka[9]); a.wg = mkptr(ka[10], ka[11]); a.wu = mkptr(ka[12], ka[13]); a.wd = mkptr(ka[14], ka[15]);
  a.out = mkptr(kb[0], kb[1]); a.ws = mkptr(kb[2], kb[3]);
  const rsrc_t rk = mkrs(a.K, 1u << 27);

  if (w == 0) DMA("buffer_load_dword", "nt", rk, junk, one, koff);
  if (r >= 8 && w >= 4) dma_piece(rk, kdst, kvo, koff);
  asm volatile("s_waitcnt lgkmcnt(0)" : "+s"(ta));
  u32 t0 = (u32)ta - 30u;
  const int phase = slot * (TAU / 32);
  Pacer pace; pace.tb = t0 + phase / 64; pace.lim = sched(1); pace.idx = 1;
  if (w == 0) {
    VMWAIT(0);
    const u32 lat = now() - t0;
    *(LAS u32*)(L + L_SH) = lat > PROBE_TH ? min(lat - PROBE_NOM, 300u) : 0u;
    if (r >= 8 && lane < 32) *(LAS u32*)(L + L_ML + lane * 4) = 0u;
  }
  if (w >= 4 && r >= 8) { pace(); dma_piece(rk, kdst + 16384, kvo, koff + PSTR); }
  if (w == 4 && r < 6) {
    const char* base = r < 2 ? a.wq : r == 2 ? a.wg : r == 3 ? a.wu : a.wo;
    const u32 off = r == 1 ? D * HD * 2 - 4 : r == 4 ? x * (512 * D * 2) : r == 5 ? (x + 1) * (512 * D * 2) - 4 : 0u;
    DMA("buffer_load_dword", "", mkrs(base + off, 4), junk, one, 0);
  }
  bar();
  const u32 sh = ldsr(L + L_SH);
  t0 += sh; pace.tb += sh; pace.lim += 3 * TW;
  asm volatile("" : "+s"(w), "+s"(r));

  if (w >= 4) {
    const int ln = (r * 4 + hw) * 28 + lane, m = ln >= 1792 ? ln - 1792 : ln;
    const u32 tw = t0 + (WARM0 + (r * 4 + hw) * (TW / 128)) / 64;
    const bool on = lane < 28;
    wait_until(tw);
    DMA("buffer_load_dword", "", mkrs(a.wq, D * HD * 2), junk, on ? (ln >> 3) * (HD * 2) + (x * 8 + (ln & 7)) * 128 : OOB, 0);
    wait_until(tw + TW / 64);
    DMA("buffer_load_dword", "", mkrs(r < 16 ? a.wg : a.wu, D * F * 2), junk, on ? (m >> 2) * (F * 2) + (((7 * x) >> 1) + (m & 3)) * 128 : OOB, 0);
    wait_until(tw + 2 * TW / 64);
    DMA("buffer_load_dword", "", mkrs(a.wo, HD * D * 2), junk, on ? x * (512 * D * 2) + ln * 128 : OOB, 0);
  }
  asm volatile("" : "+s"(w), "+s"(r));
  if (r >= 8 && w >= 4) {
#pragma nounroll
    for (int t = 2; t < NL; ++t) { pace(); dma_piece(rk, kdst + t * 16384, kvo, koff + t * PSTR); }
    early_wave(a, L, bh, s, hw, lane, pace, slot, t0);
    ROLE_END(1);
  }
  asm volatile("" : "+s"(w), "+s"(r));
  if (r < 8 && w >= 4) {
    if (service_loader(a, L, x, r, hw, lane, t0, rcode)) ffn_wave(a, L, x, r, hw, lane, t0);
    ROLE_END(2);
  }
  asm volatile("" : "+s"(w), "+s"(r));
  if (r < 8) {
    u64 tag = 0;
    if (service_front(a, L, x, r, w, lane, t0, tag)) proj_wave(a, L, x, r, w, lane, t0, tag);
    ROLE_END(3);
  }
  asm volatile("" : "+s"(w), "+s"(r));
  steady_wave(a, L, bh, s, w, lane, phase, t0);
  asm volatile(".Lfused_last:");
}

extern "C" void kernel_launch(void* const* d_in, const int* in_sizes, int n_in, void* d_out, int out_size, void* d_ws, size_t ws_size,
                              hipStream_t stream) {
  Args a;
  a.x = (const char*)d_in[0]; a.K = (const char*)d_in[1]; a.V = (const char*)d_in[2]; a.wq = (const char*)d_in[3]; a.wo = (const char*)d_in[4];
  a.wg = (const char*)d_in[6]; a.wu = (const char*)d_in[7]; a.wd = (const char*)d_in[8];
  a.out = (char*)d_out; a.ws = (char*)d_ws;
  (void)hipFuncSetAttribute((const void*)fused_decode, hipFuncAttributeMaxDynamicSharedMemorySize, L_END);
  fused_decode<<<256, 512, L_END, stream>>>(a);
}
```

```cpp
#include <hip/hip_runtime.h>
#include <cstdint>

typedef unsigned u32;
typedef unsigned long long u64;
typedef u32 v2u __attribute__((ext_vector_type(2)));
typedef u32 v4u __attribute__((ext_vector_type(4)));
typedef u32 v16u __attribute__((ext_vector_type(16)));
typedef float v2f __attribute__((ext_vector_type(2)));
typedef float v4f __attribute__((ext_vector_type(4)));
typedef short v4s __attribute__((ext_vector_type(4)));
typedef __bf16 v2b __attribute__((ext_vector_type(2)));
typedef __bf16 v8b __attribute__((ext_vector_type(8)));
typedef __amdgpu_buffer_rsrc_t rsrc_t;
#define GAS __attribute__((address_space(1)))
#define LAS __attribute__((address_space(3)))
#define DEV __device__ __forceinline__

constexpr int D = 448, F = 1792, HD = 4096;
constexpr int NP = 43;
constexpr int NL = 9, NR = 4;
constexpr int NE = NL + NR;
static_assert((NP - NE) % 3 == 0 && (NP - 1) % 3 == 0 && NR <= NL, "the steady loops handle three pieces per trip");
constexpr u32 PSTR = 6 * 16384;

constexpr int TAU = 2565;
constexpr int OFF = 600;
constexpr int TW = 3150;
constexpr int W0 = 3;
constexpr int XT = 18;
constexpr int N1 = 17;
constexpr int WSTEP = 500;
constexpr int PROBE_TH = 84;
constexpr int PROBE_NOM = 70;
constexpr int SLEW = 40;
constexpr int FLAG_T = 400;
constexpr int Q_T = 600;
constexpr int C0 = 860;
constexpr int CPER = 258;
constexpr int CN = 10;
constexpr int CWIN = 45;
constexpr int CRD = 125;
constexpr int CMIN = 8;
constexpr int CBIG = 30;
constexpr int CFLOOR = 150;
constexpr int LR_MUL = 10;
constexpr int CADD = 20;
constexpr int LR_T = 80;
constexpr int LR_MIN = 0;
constexpr int LR_ADD = 10;
constexpr int LQ_T = 15;
constexpr int WD_T = 40;
constexpr int ML_T = 108;
constexpr int O_T = 132;
constexpr int Y_D = 22;
constexpr int F_D = 23;
constexpr int ALT = 0;
DEV u32 piece_off(int t, int s) { return t * PSTR + (ALT && (t & 1) ? ((s & 1) ? -16384 : 16384) : 0); }
constexpr int TAU1 = TAU + XT * 64 / N1;
constexpr int sched(int t) { return OFF + (t >= W0 ? 3 * TW : 0) + (t < N1 ? t * TAU1 : N1 * TAU1 + (t - N1) * TAU); }
constexpr int WARM0 = sched(W0) - 3 * TW, PULL0 = WARM0 + 2 * TW + TW / 4 + 640;
constexpr u32 LAST = (sched(2 * NP - 1) + TAU / 3) / 64;
constexpr u32 TIMEOUT = 50000000u;

constexpr int AUX_NT = 2, AUX_SC1 = 16;
constexpr u32 OOB = 0x80000000u, EMPTY = 0xFFFFFFFFu, ONES = 0x3F803F80u;

constexpr u32 WS_EPOCH = 0, WS_FLAG = 4096, WS_Q = 8192, WS_ML = WS_Q + 16384, WS_O = WS_ML + 192 * 128, WS_Y = WS_O + 192 * 512;
constexpr u32 YSTR = 8 * 1792, WS_F = WS_Y + 4 * YSTR, FSTR = 7424, WS_CTL = WS_F + 28 * FSTR;
constexpr u32 CLR = 6400, WS_END = WS_ML + 64 * CLR;
static_assert(WS_CTL % 128 == 0 && WS_CTL + (CN + 1) * 512 <= WS_END, "the cleared region covers every inbox");

constexpr int L_WQ = 0, L_WO = 0, L_WG = 65536, L_WD = L_WG + 2 * 28672, L_XN = L_WD + 28672, L_HB = L_XN, L_HF = L_XN + 1792;
constexpr int L_GU = L_HF + 3584, L_ZERO = L_GU + 128, L_TB = L_ZERO + 16, L_R2 = L_TB + 8, L_YR = L_TB + 16;
constexpr int L_Q = 0, L_ML = 512, L_EM = 544, L_QOK = 576, L_TG = 580, L_CD = 584, L_CE = 588, L_CR = 640;
constexpr int L_SC = 1024, SCW = 1408, L_ACC = L_SC + 4 * SCW, L_SJ = L_ACC + 4096, L_KB = 12544;
constexpr int L_JUNK = L_KB + NL * 16384, L_SH = L_JUNK + 256, L_END = L_SH + 16;
static_assert(L_SJ + 256 + NP * 32 <= L_KB && L_YR + 2048 <= L_JUNK && L_YR % 16 == 0 && L_END <= 163840, "LDS maps");

struct Args { const char *x, *K, *V, *wq, *wo, *wg, *wu, *wd; char *out, *ws; };

DEV rsrc_t mkrs(const void* p, u32 n) { return __builtin_amdgcn_make_buffer_rsrc((void*)p, 0, n, 0x00020000); }
template <int AUX> DEV u32 ld4(rsrc_t r, u32 v, u32 s) { return __builtin_amdgcn_raw_buffer_load_b32(r, v, s, AUX); }
template <int AUX> DEV v2u ld8(rsrc_t r, u32 v, u32 s) { return __builtin_amdgcn_raw_buffer_load_b64(r, v, s, AUX); }
template <int AUX> DEV v4u ld16(rsrc_t r, u32 v, u32 s) { return __builtin_amdgcn_raw_buffer_load_b128(r, v, s, AUX); }
DEV void put4(u32 x, rsrc_t r, u32 v, u32 s) { __builtin_amdgcn_raw_buffer_store_b32(x, r, v, s, AUX_SC1); }
DEV void put8(v2u x, rsrc_t r, u32 v, u32 s) { __builtin_amdgcn_raw_buffer_store_b64(x, r, v, s, AUX_SC1); }
DEV void put16(v4u x, rsrc_t r, u32 v, u32 s) { __builtin_amdgcn_raw_buffer_store_b128(x, r, v, s, AUX_SC1); }
DEV u32 laddr(LAS char* p) { return (u32)(size_t)p; }

#define DMA(op, mods, rs, dst, vo, so)                                                                                  \
  do {                                                                                                                  \
    u32 keep_;                                                                                                          \
    asm volatile("s_mov_b32 %0, m0\n\ts_mov_b32 m0, %1\n\ts_nop 2\n\t" op " %2, %3, %4 offen " mods " lds\n\ts_mov_b32 m0, %0" \
                 : "=&s"(keep_) : "s"(dst), "v"(vo), "s"(rs), "s"(so) : "memory");                                      \
  } while (0)
#define WINDOW "s_waitcnt vmcnt(11)"
DEV void dma_piece(rsrc_t rs, u32 dst, u32 vo, u32 so) {
  u32 keep;
  asm volatile("s_mov_b32 %0, m0\n\ts_mov_b32 m0, %1\n\ts_nop 1\n\t"
               WINDOW "\n\tbuffer_load_dwordx4 %2, %3, %4 offen nt lds\n\t"
               WINDOW "\n\tbuffer_load_dwordx4 %2, %3, %4 offen offset:1024 nt lds\n\t"
               WINDOW "\n\tbuffer_load_dwordx4 %2, %3, %4 offen offset:2048 nt lds\n\t"
               WINDOW "\n\tbuffer_load_dwordx4 %2, %3, %4 offen offset:3072 nt lds\n\t"
               "s_mov_b32 m0, %0"
               : "=&s"(keep) : "s"(dst), "v"(vo), "s"(rs), "s"(so) : "memory");
}

#define FENCE() do { asm volatile("" ::: "memory"); __builtin_amdgcn_sched_barrier(0); } while (0)
#define VMWAIT(n) asm volatile("s_waitcnt vmcnt(" #n ")" ::: "memory")
#define SETTLE(n) do { __builtin_amdgcn_s_waitcnt(0); asm volatile("s_nop " #n ::: "memory"); } while (0)
#define ROLE_END(n) do { __builtin_amdgcn_s_waitcnt(0); asm volatile("s_endpgm ; role " #n ::: "memory"); } while (0)
#define INTERLEAVE(cnt, valu) _Pragma("unroll") for (int i_ = 0; i_ < cnt; ++i_) { __builtin_amdgcn_sched_group_barrier(0x8, 1, 0); __builtin_amdgcn_sched_group_barrier(0x2, valu, 0); }
DEV void bar() { __builtin_amdgcn_sched_barrier(0); asm volatile("s_waitcnt lgkmcnt(0)\n\ts_barrier" ::: "memory"); __builtin_amdgcn_sched_barrier(0); }

DEV u32 now() { return (u32)__builtin_amdgcn_s_memrealtime(); }
DEV int fine(u32 base) { return min((int)(now() - base), 1 << 24) * 64; }
DEV void wait_until(u32 t) { while ((int)(now() - t) < 0) __builtin_amdgcn_s_sleep(1); }
DEV void spin_until(u32 t) { while ((int)(now() - t) < 0) {} }
DEV bool expired(u32 t0) { return (int)(now() - t0) > (int)TIMEOUT; }

DEV float asf(u32 x) { return __uint_as_float(x); }
DEV u32 asu(float x) { return __float_as_uint(x); }
DEV float dot2(u32 a, u32 b, float c) { return __builtin_amdgcn_fdot2_f32_bf16(__builtin_bit_cast(v2b, a), __builtin_bit_cast(v2b, b), c, false); }
DEV u32 pk(float lo, float hi) { return __builtin_bit_cast(u32, __builtin_convertvector((v2f{lo, hi}), v2b)); }
DEV float lo16(u32 x) { return asf(x << 16); }
DEV float hi16(u32 x) { return asf(x & 0xffff0000u); }
DEV float rbf(float x) { return hi16(pk(x, x)); }
DEV v4f mfma(v4u a, v4u b, v4f c) { return __builtin_amdgcn_mfma_f32_16x16x32_bf16(__builtin_bit_cast(v8b, a), __builtin_bit_cast(v8b, b), c, 0, 0, 0); }
template <int C> DEV u32 dppu(u32 x) { return (u32)__builtin_amdgcn_mov_dpp((int)x, C, 0xf, 0xf, true); }
template <int C> DEV float dpp(float x) { return asf(dppu<C>(asu(x))); }
constexpr int X1 = 0xB1, X2 = 0x4E, X7 = 0x141, X15 = 0x140, ROR4 = 0x124, ROR8 = 0x128, BCAST = 0x150;
DEV v2f swap16(float a, float b) { auto s = __builtin_amdgcn_permlane16_swap(asu(a), asu(b), false, false); return v2f{asf(s[0]), asf(s[1])}; }
DEV v2f swap32(float a, float b) { auto s = __builtin_amdgcn_permlane32_swap(asu(a), asu(b), false, false); return v2f{asf(s[0]), asf(s[1])}; }
DEV float add16(float a, float b) { const v2f s = swap16(a, b); return s.x + s.y; }
DEV float add32(float a, float b) { const v2f s = swap32(a, b); return s.x + s.y; }
DEV float mul32(float a, float b) { const v2f s = swap32(a, b); return s.x * s.y; }
DEV float row_sum(float x) { x += dpp<X1>(x); x += dpp<X2>(x); x += dpp<X7>(x); x += dpp<X15>(x); return x; }
DEV float wave_sum(float x) { x = row_sum(x); x = add16(x, x); return add32(x, x); }
DEV u32 safe(u32 x) { return min(x, EMPTY - 1u); }
DEV u32 umax4(v4u v) { return max(max(v.x, v.y), max(v.z, v.w)); }
DEV v4u onehot(int t, u32 v) { return v4u{t == 0 ? v : 0u, t == 1 ? v : 0u, t == 2 ? v : 0u, t == 3 ? v : 0u}; }
DEV v4u lr16(LAS char* p) { return *(LAS v4u*)p; }
DEV u32 ldsr(LAS char* p) { return __builtin_amdgcn_readfirstlane(*(volatile LAS u32*)p); }
DEV v4u trfrag(LAS char* p0, LAS char* p1) {
  const v2u a = __builtin_bit_cast(v2u, __builtin_amdgcn_ds_read_tr16_b64_v4i16((LAS v4s*)p0));
  const v2u b = __builtin_bit_cast(v2u, __builtin_amdgcn_ds_read_tr16_b64_v4i16((LAS v4s*)p1));
  return v4u{a.x, a.y, b.x, b.y};
}


DEV u64 epoch_tag(rsrc_t rws) {
  const v2u e = ld8<AUX_SC1>(rws, WS_EPOCH, 0);
  return (((u64)(u32)__builtin_amdgcn_readfirstlane(e.y) << 32) | (u32)__builtin_amdgcn_readfirstlane(e.x)) + 1;
}
DEV bool await_flags(rsrc_t rws, u64 tag, u32 t0, int lane, u32& base) {
  v2u f; bool ok;
#pragma nounroll
  for (;;) {
    f = ld8<AUX_SC1>(rws, lane * 8, WS_FLAG);
    ok = __all(((f.y ^ (u32)(tag >> 8)) | ((f.x ^ (u32)tag) & 0xffu)) == 0);
    if (ok || expired(t0)) break;
    __builtin_amdgcn_s_sleep(64); FENCE();
  }
  FENCE();
  const float d = fminf(fmaxf((float)((int)((f.x & ~0xffu) - (t0 << 8)) >> 8), -1000.f), 1000.f);
  base = t0 + (u32)__builtin_amdgcn_readfirstlane((int)(wave_sum((lane & 7) == 7 ? 0.f : d) * (1.f / 56.f)));
  return ok;
}
DEV int round_shift(u32 raw) {
  const u32 v = raw + 1u, h = v + dppu<X1>(v), sum = (u32)__builtin_amdgcn_readfirstlane(h + dppu<X2>(h));
  return (int)min(sum & 0xffffu, 400u * 64u) + (sum >> 16 ? CFLOOR : 0);
}

struct Pacer {
  u32 tb; int lim, idx, late;
  DEV void operator()() {
    int e = late = fine(tb) - lim;
    while (e < 0) { __builtin_amdgcn_s_sleep(1); e = fine(tb) - lim; }
    lim += ++idx <= N1 ? TAU1 : TAU;
  }
};

struct Scorer {
  v4u q; LAS char* dst; bool hi8; float mx;
  DEV Scorer(LAS char* L, int w, int lane) {
    q = lr16(L + L_Q + (lane & 31) * 16);
    dst = (lane & 7) == 0 ? L + L_SC + w * SCW + (lane >> 5) * 16 + (((lane >> 4) & 1) + ((lane >> 2) & 2)) * 4 : L + L_SJ + lane * 4;
    hi8 = lane & 8; mx = -INFINITY;
  }
  DEV void operator()(v4u k0, v4u k1, v4u k2, v4u k3, int t, bool live) {
    float d0 = 0.f, d1 = 0.f, d2 = 0.f, d3 = 0.f;
#pragma unroll
    for (int i = 0; i < 4; ++i) { d0 = dot2(k0[i], q[i], d0); d1 = dot2(k1[i], q[i], d1); d2 = dot2(k2[i], q[i], d2); d3 = dot2(k3[i], q[i], d3); }
    const float e0 = add16(d0, d1), e1 = add16(d2, d3);
    const float f0 = e0 + dpp<ROR8>(e0), f1 = e1 + dpp<ROR8>(e1);
    float f = hi8 ? f1 : f0;
    f += dpp<X7>(f); f += dpp<X2>(f); f += dpp<X1>(f);
    f = live ? rbf(f) * (0.0625f * 1.4426950408889634f) : -INFINITY;
    *(LAS float*)(dst + t * 32) = f;
    mx = fmaxf(mx, f);
  }
  DEV float wave_max() {
    const float a = fmaxf(mx, dpp<ROR8>(mx));
    const v2f b = swap16(a, a); const float c = fmaxf(b.x, b.y);
    const v2f d = swap32(c, c); return fmaxf(d.x, d.y);
  }
};

DEV void await_q(LAS char* L) {
  while (ldsr(L + L_QOK) == 0u) __builtin_amdgcn_s_sleep(2);
  FENCE();
}

DEV void steady_wave(const Args& a, LAS char* L, int bh, int s, int w, int lane, int phase, u32 t0) {
  const rsrc_t rk = mkrs(a.K + ((size_t)bh << 22), 1u << 22), rv = mkrs(a.V + ((size_t)bh << 22), 1u << 22);
  const int np = s < 4 ? NP : NP - 1;
  const u32 voff = lane * 16 + w * 4096 + s * 16384;
  Pacer pace; pace.tb = t0 + phase / 64; pace.lim = sched(NE); pace.idx = NE;
  bool flags_ok = true;
  if (w == 0) {
    const rsrc_t rws = mkrs(a.ws, WS_END);
    const u64 tag = epoch_tag(rws);
    wait_until(pace.tb + FLAG_T);
    u32 tg;
    flags_ok = await_flags(rws, tag, t0, lane, tg);
    *(LAS u32*)(L + L_TG) = tg;
  }
  const int g = lane >> 4, n = lane & 15;
  v4u msk = onehot((n & 7) >> 1, (n >> 3) == (g & 1) ? ((n & 1) ? 0xffff0000u : 0x0000ffffu) : 0u);
  LAS char* pr = L + L_SC + w * SCW + (lane >> 5) * 16;
  LAS char* accw = L + L_ACC + w * 1024 + ((lane >> 5) * 16 + n) * 32 + (g & 1) * 16;
  asm volatile("" : "+v"(accw), "+v"(pr), "+v"(msk));
  __builtin_amdgcn_s_setprio(2);

  const int sq = phase / 3;
  int applied = 0, target = 0, gap = 0;
  auto follow = [&](int d) {
    if (d == 0) target = (int)ldsr(L + L_CD);
    if (w == 1) *(LAS int*)(L + L_CE) = pace.late + applied;
    const int inc = (target - applied + 3) >> 2, gs = min(max(gap, -SLEW), SLEW);
    applied += inc; gap -= gs; pace.lim += inc + gs;
  };
  v4u buf[3][4];
  auto issue = [&](int d, rsrc_t rs, u32 vo, u32 so) {
#pragma unroll
    for (int i = 0; i < 4; ++i) { buf[d][i] = ld16<AUX_NT>(rs, vo + i * 1024, so); FENCE(); }
  };

  const rsrc_t rq = mkrs(a.ws + WS_Q + bh * 512, w == 0 ? 512 : 0);
  wait_until(pace.tb + Q_T);
  FENCE();
  const v2u q0 = ld8<AUX_SC1>(rq, lane * 8, 0);
  FENCE();
  pace(); issue(0, rk, voff, piece_off(NE, s));
  pace(); issue(1, rk, voff, piece_off(NE + 1, s));
  if (w == 0) {
    *(LAS v2u*)(L + L_Q + lane * 8) = flags_ok ? q0 : v2u{EMPTY, EMPTY};
    if (flags_ok && !__all(max(q0.x, q0.y) != EMPTY)) {
#pragma nounroll
      for (;;) {
        FENCE();
        const v2u q1 = ld8<AUX_SC1>(rq, lane * 8, 0);
        *(LAS v2u*)(L + L_Q + lane * 8) = q1;
        if (__all(max(q1.x, q1.y) != EMPTY) || expired(t0)) break;
      }
    }
    FENCE();
    *(volatile LAS u32*)(L + L_QOK) = 1u;
  }
  await_q(L);
  gap = (int)(ldsr(L + L_TG) - t0) * 64;

  Scorer score(L, w, lane);
#pragma nounroll
  for (int t = NE; t < NP; t += 3) {
#pragma unroll
    for (int d = 0; d < 3; ++d) {
      const int u = t + d + 2;
      const bool isv = d > 0 && u >= NP;
      if (d < 2 && t == NP - 3) pace.lim += d == 0 ? -sq : sq - TAU / 3;
      pace();
      issue((d + 2) % 3, isv ? rv : rk, (d == 0 && u >= np) ? OOB : voff, piece_off(isv ? u - NP : u, s));
      follow(d);
      score(buf[d][0], buf[d][1], buf[d][2], buf[d][3], t + d, d < 2 || t + d < np);
      FENCE();
    }
  }

  float m = 0.f;
  v4f acc = {0.f, 0.f, 0.f, 0.f};
  auto pv = [&](int d, int t) {
    const v4f sv = *(LAS v4f*)(pr + t * 32);
    v4u pa[4];
#pragma unroll
    for (int i = 0; i < 4; ++i) { const float e = __builtin_amdgcn_exp2f(sv[i] - m); pa[i] = msk & pk(e, e); }
    FENCE();
    acc = mfma(pa[3], buf[d][3], mfma(pa[2], buf[d][2], mfma(pa[1], buf[d][1], mfma(pa[0], buf[d][0], acc))));
    asm volatile("" : "+v"(acc));
    FENCE();
  };
#pragma nounroll
  for (int t = 0; t < NP - 1; t += 3) {
#pragma unroll
    for (int d = 0; d < 3; ++d) {
      const int u = t + d + 2;
      if (d == 1 && u == NP - 1) pace.lim -= sq;
      if (d == 0 || u < np) pace();
      issue((d + 2) % 3, rv, (d == 0 || u < np) ? voff : OOB, piece_off(u, s));
      if (d == 0 && t == 0) {
        v2u em = *(volatile LAS v2u*)(L + L_EM + w * 8);
        m = score.wave_max();
        while (__builtin_amdgcn_readfirstlane(em.y) == 0u) em = *(volatile LAS v2u*)(L + L_EM + w * 8);
        m = fmaxf(m, asf(em.x));
        *(volatile LAS v2u*)(L + L_ML + w * 8) = v2u{asu(m), 1u};
        FENCE();
      }
      follow(d);
      pv(d, t + d);
    }
  }
  pv(0, NP - 1);
  *(LAS v4f*)accw = acc;
  bar();
  asm volatile("" :: "v"(buf[1][0]), "v"(buf[1][1]), "v"(buf[1][2]), "v"(buf[1][3]));
}

DEV void early_wave(const Args& a, LAS char* L, int bh, int s, int w, int lane, Pacer pace, int slot, u32 t0) {
  const rsrc_t rws = mkrs(a.ws, WS_END);
  u32 tg = 0; int ev = 0, shift = 0; bool quiet = false;
  auto tick = [&]() {
    const int k = ev >> 2, st = ev & 3;
    const u32 tk = C0 + k * CPER, dt = now() - tg;
    if (k >= CN || dt < tk + (st == 0 ? 0 : st == 1 ? CRD : CRD + 70)) return;
    if (st == 0) {
      const int id = blockIdx.x - 64, rid = id / 3, e = ((int)ldsr(L + L_CE) - shift) >> 6;
      const int v = (id == rid * 3 && e > CMIN ? min(e + CADD, 400) : 0) + (e > CBIG && (id == rid * 3 || quiet) ? 0x10000 : 0);
      if (v && dt < tk + CWIN) __builtin_amdgcn_raw_ptr_buffer_atomic_add_i32(v, rws, lane == 0 ? (rid & 3) * 128 : OOB, WS_CTL + k * 512, 0);
    } else if (st == 1) {
      DMA("buffer_load_dword", "sc1", rws, laddr(L + L_CR), lane < 4 ? lane * 128 : OOB, WS_CTL + k * 512);
    } else {
      VMWAIT(0);
      const int inc = round_shift(*(LAS u32*)(L + L_CR + (lane & 3) * 4));
      shift += inc; quiet = inc < CFLOOR + 300;
      *(LAS int*)(L + L_CD) = shift;
      ++ev;
    }
    ++ev;
  };
  {
    const rsrc_t rk = mkrs(a.K + ((size_t)bh << 22), 1u << 22);
    const u32 voff = lane * 16 + w * 4096 + s * 16384;
    v4u buf[NR][4];
#pragma unroll
    for (int t = 0; t < NR; ++t) {
      pace();
#pragma unroll
      for (int i = 0; i < 4; ++i) { asm volatile(WINDOW ::: "memory"); buf[t][i] = ld16<AUX_NT>(rk, voff + i * 1024, piece_off(NL + t, s)); FENCE(); }
    }
    VMWAIT(0);
    await_q(L);
    tg = ldsr(L + L_TG);
    Scorer score(L, w, lane);
    LAS char* kp = L + L_KB + w * 4096 + lane * 16;
#pragma nounroll
    for (int t = 0; t < NE; ++t) {
      if (t == NL) {
#pragma unroll
        for (int u = 0; u < NR * 4; ++u) *(LAS v4u*)(kp + (u >> 2) * 16384 + (u & 3) * 1024) = buf[u >> 2][u & 3];
      }
      LAS char* p = kp + (t < NL ? t : t - NL) * 16384;
      score(lr16(p), lr16(p + 1024), lr16(p + 2048), lr16(p + 3072), t, true);
      FENCE();
      if (w == 1) tick();
    }
    const float em = score.wave_max();
    FENCE();
    *(volatile LAS v2u*)(L + L_EM + w * 8) = v2u{asu(em), 1u};
  }
  if (w == 1) {
#pragma nounroll
    while (ev < 4 * CN) { tick(); __builtin_amdgcn_s_sleep(2); }
  }
  if (w != 0) return;

  v2u mw = *(volatile LAS v2u*)(L + L_ML + (lane & 3) * 8);
  while (!__all(mw.y != 0u)) { __builtin_amdgcn_s_sleep(16); mw = *(volatile LAS v2u*)(L + L_ML + (lane & 3) * 8); }
  FENCE();
  const float mi = asf(mw.x), m2 = fmaxf(mi, dpp<X1>(mi)), M = fmaxf(m2, dpp<X2>(m2));
  float l = 0.f;
#pragma nounroll
  for (int i = 0; i < 4; ++i) {
    const bool two = lane < 2 * NP - 64;
    const v4f sa = *(LAS v4f*)(L + L_SC + i * SCW + lane * 16), sb = *(LAS v4f*)(L + L_SC + i * SCW + 1024 + (two ? lane : 0) * 16);
    float l2 = 0.f;
#pragma unroll
    for (int k = 0; k < 4; ++k) { l += __builtin_amdgcn_exp2f(sa[k] - M); l2 += __builtin_amdgcn_exp2f(sb[k] - M); }
    if (two) l += l2;
  }
  l = wave_sum(l);
  const float f = __builtin_amdgcn_exp2f(mi - M) / l;
  float fac[4];
#pragma unroll
  for (int i = 0; i < 4; ++i) fac[i] = asf(__builtin_amdgcn_readlane(asu(f), i));
  const int rec = (bh & 15) * 12 + (bh >> 4) * 6 + s;
  wait_until(tg + C0 + CN * CPER);
  const int sh = (int)ldsr(L + L_CD), ahead = (31 - slot) * (TAU / 48) / 64;
  const u32 tl = tg + (sh >> 6) + LAST;
  wait_until(tl - LR_T);
  const int e = (((int)ldsr(L + L_CE) - sh) >> 6) - ahead - (s < 4 ? 0 : TAU / 96);
  if (e > LR_MIN && lane == 0)
    __hip_atomic_fetch_min((GAS u32*)(a.ws + WS_CTL + CN * 512 + (slot & 3) * 128), EMPTY - (u32)min(e * LR_MUL / 16 + LR_ADD, 400), __ATOMIC_RELAXED, __HIP_MEMORY_SCOPE_AGENT);
  wait_until(tl - ahead + 4);
  put8(v2u{safe(asu(M)), safe(asu(l))}, rws, lane == 0 ? 0u : OOB, WS_ML + rec * 128);

  u32 oso = WS_O + rec * 512, ovo = lane * 8;
  LAS char* ar = L + L_ACC + lane * 16;
  asm volatile("" : "+s"(fac[0]), "+s"(fac[1]), "+s"(fac[2]), "+s"(fac[3]), "+s"(oso));
  asm volatile("" : "+v"(ovo), "+v"(ar));
  __builtin_amdgcn_s_setprio(3);
  bar();
  const v4f a0 = *(LAS v4f*)ar, a1 = *(LAS v4f*)(ar + 1024), a2 = *(LAS v4f*)(ar + 2048), a3 = *(LAS v4f*)(ar + 3072);
  FENCE();
  const v4f o = (a0 * fac[0] + a1 * fac[1]) + (a2 * fac[2] + a3 * fac[3]);
  put8(v2u{safe(pk(o.x, o.y)), safe(pk(o.z, o.w))}, rws, ovo, oso);
}

DEV void proj_wave(const Args& a, LAS char* L, int x, int j, int w, int lane, u32 t0, u64 tag) {
  const int g = lane >> 4, n = lane & 15;
  const rsrc_t rws = mkrs(a.ws, WS_END);
  const v4f zf = {0.f, 0.f, 0.f, 0.f};
  LAS char* img = L + L_WO + w * 16384;
  v4u wf[4][4];
#pragma unroll
  for (int i = 0; i < 16; ++i) { LAS char* p = img + ((i >> 2) * 32 + g * 8 + (n >> 2)) * 128 + (i & 3) * 32 + (n & 3) * 8; wf[i >> 2][i & 3] = trfrag(p, p + 512); }
  const int prow = (n >> 3) * 6 + (n & 7), ms = (n & 7) ^ ((g & 1) * 4);
  u32 ovo[3]; LAS char* td[4];
#pragma unroll
  for (int k = 0; k < 3; ++k) ovo[k] = (k * 4 + g) * 512 + (n ^ (k * 4 + g)) * 16;
#pragma unroll
  for (int t = 0; t < 4; ++t) td[t] = (n & 7) < 6 ? img + prow * 256 + ((t * 4 + g) ^ prow) * 16 : L + L_ZERO;
  LAS char* yw = L + L_YR + w * 512 + (lane & 31) * 8 + (lane >> 5) * 4;
  LAS char* yr = L + L_YR + lane * 8;
  LAS char* mr = img + 4096 + lane * 16;
  u32 yvo = lane * 4, mvo = ms < 6 && n < 8 ? ((g >> 1) * 6 + ms) * 128 : OOB;
  float mdead = ms < 6 ? 0.f : -INFINITY;
  asm volatile("s_waitcnt lgkmcnt(0)" : "+v"(ovo[0]), "+v"(ovo[1]), "+v"(ovo[2]), "+v"(td[0]), "+v"(td[1]), "+v"(td[2]), "+v"(td[3]),
               "+v"(yw), "+v"(yr), "+v"(mr), "+v"(yvo), "+v"(mvo), "+v"(mdead) :: "memory");
  const int head = x * 2 + (w >> 1);
  u32 oso = WS_O + head * 6144 + (w & 1) * 256, mso = WS_ML + head * 1536, yso = WS_Y + w * YSTR + x * 1792 + j * 256, dst = laddr(img);
  asm volatile("" : "+s"(oso), "+s"(mso), "+s"(yso), "+s"(dst));
  auto fetch_ml = [&]() { DMA("buffer_load_dwordx4", "sc1", rws, dst + 4096, mvo, mso); };
  auto fetch_o = [&]() {
    u32 keep;
    asm volatile("s_mov_b32 %0, m0\n\ts_mov_b32 m0, %1\n\ts_nop 0\n\tbuffer_load_dwordx4 %2, %5, %6 offen sc1 lds\n\t"
                 "s_add_u32 m0, m0, 0x400\n\ts_nop 0\n\tbuffer_load_dwordx4 %3, %5, %6 offen sc1 lds\n\t"
                 "s_add_u32 m0, m0, 0x400\n\ts_nop 0\n\tbuffer_load_dwordx4 %4, %5, %6 offen sc1 lds\n\ts_mov_b32 m0, %0"
                 : "=&s"(keep) : "s"(dst), "v"(ovo[0]), "v"(ovo[1]), "v"(ovo[2]), "s"(rws), "s"(oso) : "memory", "scc");
  };
  float wv[4]; u32 mlx;
  auto weights = [&]() {
    VMWAIT(3);
    const v2u ml = *(LAS v2u*)mr;
    const float m = asf(ml.x) + mdead;
    float M = fmaxf(m, dpp<X1>(m)); M = fmaxf(M, dpp<X2>(M)); M = fmaxf(M, dpp<X7>(M));
    const float e = __builtin_amdgcn_exp2f(m - M) * asf(ml.y);
    float sum = e + dpp<X1>(e); sum += dpp<X2>(sum); sum += dpp<X7>(sum);
    const float wt = e * __builtin_amdgcn_rcpf(sum);
    wv[0] = dpp<BCAST>(wt); wv[1] = dpp<BCAST + 1>(wt); wv[2] = dpp<BCAST + 2>(wt); wv[3] = dpp<BCAST + 3>(wt);
    mlx = max(ml.x, ml.y);
    asm volatile("s_waitcnt vmcnt(0)" : "+v"(wv[0]), "+v"(wv[1]), "+v"(wv[2]), "+v"(wv[3]), "+v"(mlx) :: "memory");
  };
  u32 tb = ldsr(L + L_TB);
  wait_until(tb + LQ_T);
  FENCE();
  const u32 lr = ld4<AUX_SC1>(rws, (lane & 3) * 128, WS_CTL + CN * 512), lr2 = min(lr, dppu<X1>(lr));
  tb += min(EMPTY - (u32)__builtin_amdgcn_readfirstlane(min(lr2, dppu<X2>(lr2))), 400u);
  wait_until(tb + ML_T);
  fetch_ml();
  spin_until(tb + O_T);
  fetch_o();
  weights();
  bool give_up = false;
#pragma nounroll
  for (;;) {
    FENCE();
    v4u af[4];
#pragma unroll
    for (int t = 0; t < 4; ++t) af[t] = lr16(td[t]);
    FENCE();
    auto tile = [&](int c) { return mfma(af[3], wf[3][c], mfma(af[2], wf[2][c], mfma(af[1], wf[1][c], mfma(af[0], wf[0][c], zf)))); };
    auto wsum = [&](v4f v) { return fmaf(v.w, wv[3], fmaf(v.z, wv[2], fmaf(v.y, wv[1], v.x * wv[0]))); };
    v4f y0 = tile(0), y1 = tile(1);
    const u32 mx = max(max(max(umax4(af[0]), umax4(af[1])), max(umax4(af[2]), umax4(af[3]))), mlx);
    INTERLEAVE(8, 2)
    asm volatile("" : "+v"(y0), "+v"(y1));
    if (__builtin_expect(__all(mx != EMPTY) || give_up, 1)) {
      FENCE();
      v4f y2 = tile(2); const float s0 = wsum(y0);
      INTERLEAVE(4, 1)
      asm volatile("" : "+v"(y2));
      FENCE();
      v4f y3 = tile(3); const float e0 = add16(s0, wsum(y1));
      INTERLEAVE(4, 2)
      asm volatile("" : "+v"(y3));
      FENCE();
      *(LAS float*)yw = e0;
      const float s2 = wsum(y2);
      FENCE();
      *(LAS float*)(yw + 256) = add16(s2, wsum(y3));
      break;
    }
    FENCE();
    fetch_ml(); fetch_o();
    give_up = expired(t0);
    weights();
  }
  bar();
  const v2f u = (*(LAS v2f*)yr + *(LAS v2f*)(yr + 512)) + (*(LAS v2f*)(yr + 1024) + *(LAS v2f*)(yr + 1536));
  put4(safe(pk(u.x, u.y)), rws, yvo, yso);
  if (w == 0) {
    LAS char* sr = lane < 56 ? L + L_HB + lane * 16 : L + L_ZERO;
    const int step = lane < 56 ? D * 2 : 0;
    bar();
    const v4u h0 = lr16(sr), h1 = lr16(sr + step);
    float s0 = 0.f, s1 = 0.f, q0 = 0.f, q1 = 0.f;
#pragma unroll
    for (int i = 0; i < 4; ++i) { s0 = dot2(h0[i], ONES, s0); s1 = dot2(h1[i], ONES, s1); q0 = dot2(h0[i], h0[i], q0); q1 = dot2(h1[i], h1[i], q1); }
    const float st = row_sum(add16(add32(s0, s1), add32(q0, q1))) * (1.f / D);
    const float var = add16((g & 1) ? st : -st * st, 0.f);
    if ((lane & 31) == 0) *(LAS float*)(L + L_R2 + (lane >> 5) * 4) = __builtin_amdgcn_rcpf(fmaxf(var, 0.f) + 1e-5f);
    bar();
    put8(v2u{(u32)tag, (u32)(tag >> 32)}, rws, (lane == 0 && x == 0 && j == 0) ? WS_EPOCH : OOB, 0);
  }
}

DEV void ffn_wave(const Args& a, LAS char* L, int x, int j, int w, int lane, u32 t0) {
  const int g = lane >> 4, n = lane & 15, fj = x * 7 + j;
  const rsrc_t rws = mkrs(a.ws, WS_END), rd = mkrs(a.wd, D * F * 2), ro = mkrs(a.out, 2 * D * 2);
  const v4f zf = {0.f, 0.f, 0.f, 0.f};
  const v4u z4 = {0u, 0u, 0u, 0u}, o4 = {ONES, ONES, ONES, ONES};
  v4u guf[14], wdf[7];
  v4f cs = zf;
#pragma unroll
  for (int t = 0; t < 14; ++t) {
    LAS char* p = L + L_WG + ((n >> 1) & 1) * 28672 + (t * 32 + g * 8 + (n >> 2)) * 64 + w * 16 + (n & 1) * 8;
    guf[t] = trfrag(p, p + 256);
    cs = mfma(guf[t], o4, cs);
  }
  cs *= -1.f / D;
  const int pc = w * 32 + (g >> 1) * 16 + n, hv = pc < 112 ? (g & 1) * D + pc * 4 : 2 * D;
  const v2u xx = ld8<0>(mkrs(a.x, 2 * D * 2), hv * 2, 0);
  v4f xc = {lo16(xx.x), hi16(xx.x), lo16(xx.y), hi16(xx.y)};
  const v4u dg = onehot(n & 3, (n & 4) ? 0x3F800000u : 0x00003F80u);
  v4u sel0 = n < 8 ? dg : z4, sel1 = n < 8 ? z4 : dg, on0 = (n & 1) ? z4 : o4, on1 = (n & 1) ? o4 : z4;
  const int tA = w * 7 + g, tB = w * 7 + 4 + g;
  const bool rA = tA == fj, rB = tB == fj && g < 3;
  LAS char* hA = L + (rA ? L_HF + (tA * 16 + n) * 4 : L_ZERO);
  LAS char* hB = L + (rB ? L_HF + (tB * 16 + n) * 4 : L_ZERO);
  LAS char* hA1 = hA + (rA ? D * 4 : 0);
  LAS char* hB1 = hB + (rB ? D * 4 : 0);
  LAS char* ga[4];
#pragma unroll
  for (int v = 0; v < 4; ++v) ga[v] = L + ((n >> 2) == v && (n & 2) == 0 ? L_GU + (n & 1) * 64 + g * 16 : L_ZERO);
  LAS char* hbw = L + (pc < 112 ? L_HB + hv * 2 : L_JUNK);
  LAS char* hfw = L + (pc < 112 ? L_HF + hv * 4 : L_JUNK);
  LAS char* hr = L + L_HB + (n & 1) * (D * 2) + g * 16;
  LAS char* s0r = lane < 56 ? L + L_HB + lane * 16 : L + L_ZERO;
  LAS char* s1r = lane < 56 ? L + L_HB + D * 2 + lane * 16 : L + L_ZERO;
  LAS char* gw = L + (n < 2 ? L_GU + n * 64 + w * 16 + (g & 1) * 8 + (g >> 1) * 4 : L_JUNK + lane * 4);
  u32 yo0 = g * 1792 + (w * 32 + n) * 16, yo1 = w < 3 ? yo0 + 256 : OOB;
  u32 fo0 = tA * FSTR + n * 8, fo1 = g < 3 ? tB * FSTR + n * 8 : OOB;
  u32 fr = (lane >> 2) * 128 + (lane & 3) * 16, fr3 = lane < 32 ? fr + 2048 : OOB;
  u32 oo = n < 4 ? (g >> 1) * (D * 2) + (n * 2 + (g & 1)) * 2 : OOB;
  u32 ys = WS_Y + (x & 3) * YSTR, ys1 = ys + 4 * 1792, fs = WS_F + fj * 128;
  u32 gs = WS_F + (fj >> 1) * FSTR + (fj & 1) * 64, gs1 = gs + 2048, gs2 = gs + 4096, os = fj * 16;
  asm volatile("" : "+s"(ys), "+s"(ys1), "+s"(fs), "+s"(gs), "+s"(gs1), "+s"(gs2), "+s"(os));
  asm volatile("" : "+v"(cs), "+v"(xc), "+v"(sel0), "+v"(sel1), "+v"(on0), "+v"(on1), "+v"(yo0), "+v"(yo1), "+v"(fo0), "+v"(fo1), "+v"(fr), "+v"(fr3), "+v"(oo));
  asm volatile("" : "+v"(hA), "+v"(hB), "+v"(hA1), "+v"(hB1), "+v"(ga[0]), "+v"(ga[1]), "+v"(ga[2]), "+v"(ga[3]), "+v"(hbw), "+v"(hfw), "+v"(hr), "+v"(s0r), "+v"(s1r), "+v"(gw));

  if (w == 1) __builtin_amdgcn_raw_buffer_store_b16((unsigned short)0, ro, oo, os, 0);
  if (w == 2) DMA("buffer_load_dword", "nt", rd, laddr(L + L_JUNK), lane < 2 ? lane * (32 * D * 2 - 4) : OOB, fj * (32 * D * 2));
  SETTLE(1);
  wait_until(ldsr(L + L_TB) + WD_T);
#pragma unroll
  for (int i = 0; i < 7; ++i) DMA("buffer_load_dwordx4", "", rd, laddr(L + L_WD) + w * 1024 + i * 4096, lane * 16 + w * 1024 + fj * (32 * D * 2), i * 4096);
  VMWAIT(0);
  bar();

  spin_until(now() + Y_D);
  auto load_y = [&](v4u (&d)[4]) {
    d[0] = ld16<AUX_SC1>(rws, yo0, ys); d[1] = ld16<AUX_SC1>(rws, yo0, ys1); d[2] = ld16<AUX_SC1>(rws, yo1, ys); d[3] = ld16<AUX_SC1>(rws, yo1, ys1);
    FENCE();
  };
  auto make_h = [&](v4u (&d)[4]) {
    const v4f h = mfma(sel1, d[3], mfma(sel1, d[2], mfma(sel0, d[1], mfma(sel0, d[0], xc))));
    const u32 mx = max(max(umax4(d[0]), umax4(d[1])), max(umax4(d[2]), umax4(d[3])));
    INTERLEAVE(4, 2)
    *(LAS v2u*)hbw = v2u{pk(h.x, h.y), pk(h.z, h.w)};
    *(LAS v4f*)hfw = h;
    return __all(mx != EMPTY);
  };
  v4u yl[4];
  load_y(yl);
#pragma unroll
  for (int t = 0; t < 7; ++t) { LAS char* p = L + L_WD + (g * 8 + (n >> 2)) * (D * 2) + (w * 7 + t) * 32 + (n & 3) * 8; wdf[t] = trfrag(p, p + 4 * D * 2); }
  asm volatile("s_waitcnt lgkmcnt(0)" ::: "memory");
  FENCE();
  if (__builtin_expect(!make_h(yl), 0)) {
#pragma nounroll
    for (;;) {
      FENCE();
      v4u yb[4];
      load_y(yb);
      if (make_h(yb) || expired(t0)) break;
    }
  }
  bar();

  v4u hf[14];
#pragma unroll
  for (int t = 0; t < 3; ++t) hf[t] = lr16(hr + t * 64);
  const v4u h0 = lr16(s0r), h1 = lr16(s1r);
#pragma unroll
  for (int t = 3; t < 10; ++t) hf[t] = lr16(hr + t * 64);
  FENCE();
  v4f gu = mfma(guf[2], hf[2], mfma(guf[1], hf[1], mfma(guf[0], hf[0], zf)));
  FENCE();
#pragma unroll
  for (int t = 10; t < 14; ++t) hf[t] = lr16(hr + t * 64);
  const v4f ms = mfma(h1, on1, mfma(h0, on0, zf));
  FENCE();
#pragma unroll
  for (int t = 3; t < 14; ++t) gu = mfma(guf[t], hf[t], gu);
  float sm = (ms.x + ms.y) + (ms.z + ms.w);
  sm = add16(sm, sm); sm = add32(sm, sm);
  INTERLEAVE(11, 1)
  const v4f c = gu + sm * cs;
  *(LAS u32*)gw = pk(mul32(c.x, c.z), mul32(c.y, c.w));
  bar();

  const v4u g0 = lr16(ga[0]), g1 = lr16(ga[1]), g2 = lr16(ga[2]), g3 = lr16(ga[3]);
  const v2f r2 = *(LAS v2f*)(L + L_R2);
  const v2f resA = {*(LAS float*)hA, *(LAS float*)hA1}, resB = {*(LAS float*)hB, *(LAS float*)hB1};
  FENCE();
  const v4f dA = mfma(g3, wdf[3], mfma(g2, wdf[2], mfma(g1, wdf[1], mfma(g0, wdf[0], zf))));
  FENCE();
  const v4f dB = mfma(g2, wdf[6], mfma(g1, wdf[5], mfma(g0, wdf[4], zf)));
  FENCE();
  const v2f oA = v2f{dA.x, dA.y} * r2 + resA;
  put8(v2u{safe(asu(oA.x)), safe(asu(oA.y))}, rws, fo0, fs);
  FENCE();
  const v2f oB = v2f{dB.x, dB.y} * r2 + resB;
  put8(v2u{safe(asu(oB.x)), safe(asu(oB.y))}, rws, fo1, fs);

  if (w == 1) {
    spin_until(now() + F_D);
    float o;
#pragma nounroll
    for (;;) {
      const v4u f0 = ld16<AUX_SC1>(rws, fr, gs), f1 = ld16<AUX_SC1>(rws, fr, gs1), f2 = ld16<AUX_SC1>(rws, fr, gs2), f3 = ld16<AUX_SC1>(rws, fr3, gs2);
      FENCE();
      const v4f t = ((__builtin_bit_cast(v4f, f0) + __builtin_bit_cast(v4f, f1)) + __builtin_bit_cast(v4f, f2)) + __builtin_bit_cast(v4f, f3);
      o = add16(add32(t.x, t.y), add32(t.z, t.w));
      o += dpp<ROR4>(o); o += dpp<ROR8>(o);
      asm volatile("" : "+v"(o));
      if (__all(o == o) || __all(max(max(umax4(f0), umax4(f1)), max(umax4(f2), umax4(f3))) != EMPTY) || expired(t0)) break;
      FENCE();
    }
    __builtin_amdgcn_raw_buffer_store_b16((unsigned short)pk(o, o), ro, oo, os, 0);
  }
}

DEV bool service_loader(const Args& a, LAS char* L, int x, int j, int w, int lane, u32 t0, rsrc_t rcode) {
  int due = PULL0 + w * (WSTEP / 4);
  auto run = [&](rsrc_t rs, u32 vo, u32 sstep, u32 dst, u32 dstep, int lo, int hi) {
#pragma nounroll
    for (int i = lo; i < hi; ++i) {
      while (fine(t0) < due) __builtin_amdgcn_s_sleep(1);
      DMA("buffer_load_dwordx4", "", rs, dst + i * dstep, vo, i * sstep);
      due += WSTEP;
    }
  };
  run(mkrs(a.wq, D * HD * 2), (w * 8 + (lane >> 3)) * (HD * 2) + ((lane & 7) ^ ((lane >> 3) & 2) ^ ((w & 1) << 2)) * 16 + (x * 8 + j) * 128,
      32 * HD * 2, laddr(L + L_WQ) + w * 1024, 4096, 0, 14);
  VMWAIT(0);
  bar();
  if (j == 7) {
    if (w < 2) {
      wait_until(t0 + (w ? 1200 : 640));
      DMA("buffer_load_dword", "nt", mkrs(w ? a.V : a.K, 1u << 27), laddr(L + L_JUNK), lane < 8 ? (lane << 21) + 2048 : OOB, x << 24);
    } else if (w == 2) {
#pragma nounroll
      for (int k = 0; k < 9; ++k) {
        wait_until(t0 + (k < 3 ? 600 : k < 6 ? 1700 : 3300));
        DMA("buffer_load_dword", "sc1", rcode, laddr(L + L_JUNK), lane * 128 + (k % 3) * 8192, 0);
      }
    }
    return false;
  }
  run(mkrs((w & 1) ? a.wu : a.wg, D * F * 2), ((w >> 1) * 16 + (lane >> 2)) * (F * 2) + (lane & 3) * 16 + (x * 7 + j) * 64,
      32 * F * 2, laddr(L + L_WG) + (w & 1) * 28672 + (w >> 1) * 1024, 2048, 0, 14);
  bar();
  run(mkrs(a.wo, HD * D * 2), (x * 512 + w * 8 + (lane >> 3)) * (D * 2) + (lane & 7) * 16 + j * 128, 32 * D * 2, laddr(L + L_WO) + w * 1024, 4096, 0, 16);
  VMWAIT(0);
  bar();
  return true;
}

DEV bool service_front(const Args& a, LAS char* L, int x, int j, int w, int lane, u32 t0, u64& tag) {
  const int g = lane >> 4, n = lane & 15, id = x * 8 + j;
  const rsrc_t rws = mkrs(a.ws, WS_END);
  if (w == 3) {
    const v4u e4 = {EMPTY, EMPTY, EMPTY, EMPTY};
    const u32 co = WS_ML + id * CLR;
#pragma unroll
    for (int i = 0; i < 6; ++i) put16(e4, rws, lane * 16 + (i & 3) * 1024, co + (i >> 2) * 4096);
    put16(e4, rws, lane < 16 ? co + 6144 + lane * 16 : lane < 32 ? WS_Q + id * 128 + ((lane >> 3) & 1) * (HD * 2) + (lane & 7) * 16 : OOB, 0);
    tag = epoch_tag(rws);
    VMWAIT(0);
    put8(v2u{(t0 << 8) | ((u32)tag & 0xffu), (u32)(tag >> 8)}, rws, lane == 0 ? WS_FLAG + id * 8 : OOB, 0);
  } else if (w == 2) {
    if (lane < 4) *(LAS u32*)(L + L_ZERO + lane * 4) = 0u;
  } else {
    const v4u xr = ld16<0>(mkrs(a.x, 2 * D * 2), lane < 56 ? lane * 16 : OOB, w * (D * 2));
    tag = epoch_tag(rws);
    float v[8] = {lo16(xr.x), hi16(xr.x), lo16(xr.y), hi16(xr.y), lo16(xr.z), hi16(xr.z), lo16(xr.w), hi16(xr.w)};
    const float mean = wave_sum(((v[0] + v[1]) + (v[2] + v[3])) + ((v[4] + v[5]) + (v[6] + v[7]))) / D;
    float ss = 0.f;
#pragma unroll
    for (int i = 0; i < 8; ++i) ss = fmaf(v[i] - mean, v[i] - mean, ss);
    const float sd = rbf(sqrtf(rbf(rbf(wave_sum(lane < 56 ? ss : 0.f) / D) + 1e-5f))), mb = rbf(mean);
#pragma unroll
    for (int i = 0; i < 8; ++i) v[i] = rbf(v[i] - mb) / sd;
    if (lane < 56) *(LAS v4u*)(L + L_XN + w * (D * 2) + lane * 16) = v4u{pk(v[0], v[1]), pk(v[2], v[3]), pk(v[4], v[5]), pk(v[6], v[7])};
  }
  bar();
  v4f qa = {0.f, 0.f, 0.f, 0.f};
#pragma unroll
  for (int t = 0; t < 14; ++t) {
    LAS char* p = L + L_WQ + (t * 32 + g * 8 + (n >> 2)) * 128 + (w ^ (n >> 3) ^ ((g & 1) << 1)) * 32 + (n & 3) * 8;
    qa = mfma(lr16(L + L_XN + (n & 1) * (D * 2) + t * 64 + g * 16), trfrag(p, p + 512), qa);
  }
  const float qv = (g & 1) ? qa.y : qa.x;
  put4(safe(pk(qv, dpp<X1>(qv))), rws, g < 2 && !(n & 1) ? g * (HD * 2) + n * 2 : OOB, WS_Q + id * 128 + w * 32);
  if (j == 7) return false;
  bar();
  if (w == 3) {
    u32 tg;
    if (!await_flags(rws, tag, t0, lane, tg)) *(LAS u32*)(L + L_ZERO) = EMPTY - 1u;
    int shift = 0;
#pragma nounroll
    for (int k = 0; k < CN; ++k) { wait_until(tg + C0 + k * CPER + CRD); FENCE(); shift += round_shift(ld4<AUX_SC1>(rws, (lane & 3) * 128, WS_CTL + k * 512)); }
    *(LAS u32*)(L + L_TB) = tg + (shift >> 6) + LAST;
  }
  bar();
  return true;
}

DEV char* mkptr(u32 lo, u32 hi) { return (char*)(((u64)hi << 32) | lo); }

__global__ void __launch_bounds__(512, 2) fused_decode(Args) {
  extern __shared__ __attribute__((aligned(16))) char smem[];
  LAS char* L = (LAS char*)smem;
  v16u ka; v4u kb; u64 pc; u32 code_bytes;
  asm volatile("s_load_dwordx16 %0, %4, 0x0\n\ts_load_dwordx4 %1, %4, 0x40\n\ts_getpc_b64 %2\n.Lfused_first:\n\ts_mov_b32 %3, .Lfused_last-.Lfused_first"
               : "=&s"(ka), "=&s"(kb), "=&s"(pc), "=&s"(code_bytes) : "s"(__builtin_amdgcn_kernarg_segment_ptr()));
  const rsrc_t rcode = mkrs((const char*)pc, code_bytes);
  const u32 junk = laddr(L + L_JUNK);
  if ((blockIdx.x >> 3) == 7) DMA("buffer_load_dword", "", rcode, junk, threadIdx.x * 64, 0);

  const int lane = threadIdx.x & 63, x = blockIdx.x & 7;
  int w = __builtin_amdgcn_readfirstlane(threadIdx.x >> 6), r = blockIdx.x >> 3;
  const int hw = w & 3, qd = r >= 8 ? ((r - 8) * 43) >> 8 : r & 3, s = r - 8 - qd * 6, bh = x * 4 + qd, slot = qd * 8 + x;
  u32 kvo = lane * 16 + hw * 4096 + s * 16384, one = lane == 0 ? 0u : OOB;
  u32 kdst = laddr(L + L_KB) + hw * 4096, koff = (u32)bh << 22;
  u64 ta;
  asm volatile("" : "+v"(kvo), "+v"(one));
  asm volatile("s_waitcnt lgkmcnt(0)\n\ts_memrealtime %4" : "+s"(ka), "+s"(kb), "+s"(kdst), "+s"(koff), "=&s"(ta));
  Args a;
  a.x = mkptr(ka[0], ka[1]); a.K = mkptr(ka[2], ka[3]); a.V = mkptr(ka[4], ka[5]); a.wq = mkptr(ka[6], ka[7]);
  a.wo = mkptr(ka[8], ka[9]); a.wg = mkptr(ka[10], ka[11]); a.wu = mkptr(ka[12], ka[13]); a.wd = mkptr(ka[14], ka[15]);
  a.out = mkptr(kb[0], kb[1]); a.ws = mkptr(kb[2], kb[3]);
  const rsrc_t rk = mkrs(a.K, 1u << 27);

  if (w == 0) DMA("buffer_load_dword", "nt", rk, junk, one, koff);
  if (r >= 8 && w >= 4) dma_piece(rk, kdst, kvo, koff);
  asm volatile("s_waitcnt lgkmcnt(0)" : "+s"(ta));
  u32 t0 = (u32)ta - 30u;
  const int phase = slot * (TAU / 32);
  Pacer pace; pace.tb = t0 + phase / 64; pace.lim = sched(1); pace.idx = 1;
  if (w == 0) {
    VMWAIT(0);
    const u32 lat = now() - t0;
    *(LAS u32*)(L + L_SH) = lat > PROBE_TH ? min(lat - PROBE_NOM, 300u) : 0u;
    if (r >= 8 && lane < 32) *(LAS u32*)(L + L_ML + lane * 4) = 0u;
  }
  if (w >= 4 && r >= 8) { pace(); dma_piece(rk, kdst + 16384, kvo, koff + piece_off(1, s)); }
  if (w == 4 && r < 6) {
    const char* base = r < 2 ? a.wq : r == 2 ? a.wg : r == 3 ? a.wu : a.wo;
    const u32 off = r == 1 ? D * HD * 2 - 4 : r == 4 ? x * (512 * D * 2) : r == 5 ? (x + 1) * (512 * D * 2) - 4 : 0u;
    DMA("buffer_load_dword", "", mkrs(base + off, 4), junk, one, 0);
  }
  bar();
  const u32 sh = ldsr(L + L_SH);
  t0 += sh; pace.tb += sh;
  asm volatile("" : "+s"(w), "+s"(r));

  if (w >= 4) {
    if (r >= 8) {
#pragma nounroll
      for (int t = 2; t < W0; ++t) { pace(); dma_piece(rk, kdst + t * 16384, kvo, koff + piece_off(t, s)); }
    }
    pace.lim += 3 * TW;
    const int ln = (r * 4 + hw) * 28 + lane, m = ln >= 1792 ? ln - 1792 : ln;
    const u32 tw = t0 + (WARM0 + (r * 4 + hw) * (TW / 128)) / 64;
    const bool on = lane < 28;
    wait_until(tw);
    DMA("buffer_load_dword", "", mkrs(a.wq, D * HD * 2), junk, on ? (ln >> 3) * (HD * 2) + (x * 8 + (ln & 7)) * 128 : OOB, 0);
    wait_until(tw + TW / 64);
    DMA("buffer_load_dword", "", mkrs(r < 16 ? a.wg : a.wu, D * F * 2), junk, on ? (m >> 2) * (F * 2) + (((7 * x) >> 1) + (m & 3)) * 128 : OOB, 0);
    wait_until(tw + 2 * TW / 64);
    DMA("buffer_load_dword", "", mkrs(a.wo, HD * D * 2), junk, on ? x * (512 * D * 2) + ln * 128 : OOB, 0);
  }
  asm volatile("" : "+s"(w), "+s"(r));
  if (r >= 8 && w >= 4) {
#pragma nounroll
    for (int t = W0; t < NL; ++t) { pace(); dma_piece(rk, kdst + t * 16384, kvo, koff + piece_off(t, s)); }
    early_wave(a, L, bh, s, hw, lane, pace, slot, t0);
    ROLE_END(1);
  }
  asm volatile("" : "+s"(w), "+s"(r));
  if (r < 8 && w >= 4) {
    if (service_loader(a, L, x, r, hw, lane, t0, rcode)) ffn_wave(a, L, x, r, hw, lane, t0);
    ROLE_END(2);
  }
  asm volatile("" : "+s"(w), "+s"(r));
  if (r < 8) {
    u64 tag = 0;
    if (service_front(a, L, x, r, w, lane, t0, tag)) proj_wave(a, L, x, r, w, lane, t0, tag);
    ROLE_END(3);
  }
  asm volatile("" : "+s"(w), "+s"(r));
  steady_wave(a, L, bh, s, w, lane, phase, t0);
  asm volatile(".Lfused_last:");
}

extern "C" void kernel_launch(void* const* d_in, const int* in_sizes, int n_in, void* d_out, int out_size, void* d_ws, size_t ws_size,
                              hipStream_t stream) {
  Args a;
  a.x = (const char*)d_in[0]; a.K = (const char*)d_in[1]; a.V = (const char*)d_in[2]; a.wq = (const char*)d_in[3]; a.wo = (const char*)d_in[4];
  a.wg = (const char*)d_in[6]; a.wu = (const char*)d_in[7]; a.wd = (const char*)d_in[8];
  a.out = (char*)d_out; a.ws = (char*)d_ws;
  (void)hipFuncSetAttribute((const void*)fused_decode, hipFuncAttributeMaxDynamicSharedMemorySize, L_END);
  fused_decode<<<256, 512, L_END, stream>>>(a);
}
```

```cpp
#include <hip/hip_runtime.h>
#include <cstdint>

typedef unsigned u32;
typedef unsigned long long u64;
typedef u32 v2u __attribute__((ext_vector_type(2)));
typedef u32 v4u __attribute__((ext_vector_type(4)));
typedef u32 v16u __attribute__((ext_vector_type(16)));
typedef float v2f __attribute__((ext_vector_type(2)));
typedef float v4f __attribute__((ext_vector_type(4)));
typedef short v4s __attribute__((ext_vector_type(4)));
typedef __bf16 v2b __attribute__((ext_vector_type(2)));
typedef __bf16 v8b __attribute__((ext_vector_type(8)));
typedef __amdgpu_buffer_rsrc_t rsrc_t;
#define GAS __attribute__((address_space(1)))
#define LAS __attribute__((address_space(3)))
#define DEV __device__ __forceinline__

constexpr int D = 448, F = 1792, HD = 4096;
constexpr int NP = 43;
constexpr int NL = 9, NR = 4;
constexpr int NE = NL + NR;
static_assert((NP - NE) % 3 == 0 && (NP - 1) % 3 == 0 && NR <= NL, "the steady loops handle three pieces per trip");
constexpr u32 PSTR = 6 * 16384;

constexpr int TAU = 2565;
constexpr int OFF = 600;
constexpr int TW = 3150;
constexpr int W0 = 3;
constexpr int XT = 24;
constexpr int N1 = 17;
constexpr int WSTEP = 500;
constexpr int PROBE_TH = 84;
constexpr int PROBE_NOM = 70;
constexpr int SLEW = 40;
constexpr int FLAG_T = 400;
constexpr int Q_T = 600;
constexpr int C0 = 860;
constexpr int CPER = 258;
constexpr int CN = 10;
constexpr int CWIN = 45;
constexpr int CRD = 125;
constexpr int CMIN = 8;
constexpr int CBIG = 30;
constexpr int CFLOOR = 150;
constexpr int LR_MUL = 10;
constexpr int CADD = 20;
constexpr int LR_T = 80;
constexpr int LR_MIN = 0;
constexpr int LR_ADD = 10;
constexpr int LQ_T = 15;
constexpr int WD_T = 40;
constexpr int ML_T = 108;
constexpr int O_T = 132;
constexpr int Y_D = 23;
constexpr int F_D = 17;
constexpr int ALT = 0;
constexpr int NEAR_XOR = 0;
DEV u32 piece_off(int t, int s) { return t * PSTR + (ALT && (t & 1) ? ((s & 1) ? -16384 : 16384) : 0); }
constexpr int TAU1 = TAU + XT * 64 / N1;
constexpr int sched(int t) { return OFF + (t >= W0 ? 3 * TW : 0) + (t < N1 ? t * TAU1 : N1 * TAU1 + (t - N1) * TAU); }
constexpr int WARM0 = sched(W0) - 3 * TW, PULL0 = WARM0 + 2 * TW + TW / 4 + 640;
constexpr u32 LAST = (sched(2 * NP - 1) + TAU / 3) / 64;
constexpr u32 TIMEOUT = 50000000u;

constexpr int AUX_NT = 2, AUX_SC1 = 16;
constexpr u32 OOB = 0x80000000u, EMPTY = 0xFFFFFFFFu, ONES = 0x3F803F80u;

constexpr u32 WS_EPOCH = 0, WS_FLAG = 4096, WS_Q = 8192, UNIT = 16384, HALF = 8192;
constexpr u32 WS_O = 2 * UNIT, WS_Y = WS_O + 8 * UNIT, WS_F = WS_Y + 4 * UNIT, WS_PING = WS_F + 14 * UNIT, WS_VOTE = WS_PING + UNIT, WS_END = WS_PING + 4 * UNIT;
constexpr u32 ML_IN_O = 6144, CLR = (WS_PING - WS_O) / 64;
constexpr u32 ctl_off(int k) { return WS_F + k * HALF + 7168; }
static_assert(WS_END == 524288 && CLR == 6656 && CN < 28, "workspace window");
DEV u32 o_block(int head, u32 near) { return WS_O + (head & 7) * UNIT + ((near >> (head >> 1)) & 1) * HALF; }
DEV u32 f_block(int tile, u32 near) { return WS_F + (tile >= 14 ? tile - 14 : tile) * UNIT + ((near >> ((tile * 74) >> 8)) & 1) * HALF; }

constexpr int L_WQ = 0, L_WO = 0, L_WG = 65536, L_WD = L_WG + 2 * 28672, L_XN = L_WD + 28672, L_HB = L_XN, L_HF = L_XN + 1792;
constexpr int L_GU = L_HF + 3584, L_ZERO = L_GU + 128, L_TB = L_ZERO + 16, L_NS = L_TB + 4, L_R2 = L_TB + 8, L_YR = L_TB + 16;
constexpr int L_Q = 0, L_ML = 512, L_EM = 544, L_QOK = 576, L_TG = 580, L_CD = 584, L_CE = 588, L_CR = 640;
constexpr int L_SC = 1024, SCW = 1408, L_ACC = L_SC + 4 * SCW, L_SJ = L_ACC + 4096, L_KB = 12544;
constexpr int L_JUNK = L_KB + NL * 16384, L_SH = L_JUNK + 256, L_END = L_SH + 16;
static_assert(L_SJ + 256 + NP * 32 <= L_KB && L_YR + 2048 <= L_JUNK && L_YR % 16 == 0 && L_END <= 163840, "LDS maps");

struct Args { const char *x, *K, *V, *wq, *wo, *wg, *wu, *wd; char *out, *ws; };

DEV rsrc_t mkrs(const void* p, u32 n) { return __builtin_amdgcn_make_buffer_rsrc((void*)p, 0, n, 0x00020000); }
template <int AUX> DEV u32 ld4(rsrc_t r, u32 v, u32 s) { return __builtin_amdgcn_raw_buffer_load_b32(r, v, s, AUX); }
template <int AUX> DEV v2u ld8(rsrc_t r, u32 v, u32 s) { return __builtin_amdgcn_raw_buffer_load_b64(r, v, s, AUX); }
template <int AUX> DEV v4u ld16(rsrc_t r, u32 v, u32 s) { return __builtin_amdgcn_raw_buffer_load_b128(r, v, s, AUX); }
DEV void put4(u32 x, rsrc_t r, u32 v, u32 s) { __builtin_amdgcn_raw_buffer_store_b32(x, r, v, s, AUX_SC1); }
DEV void put8(v2u x, rsrc_t r, u32 v, u32 s) { __builtin_amdgcn_raw_buffer_store_b64(x, r, v, s, AUX_SC1); }
DEV void put16(v4u x, rsrc_t r, u32 v, u32 s) { __builtin_amdgcn_raw_buffer_store_b128(x, r, v, s, AUX_SC1); }
DEV u32 laddr(LAS char* p) { return (u32)(size_t)p; }

#define DMA(op, mods, rs, dst, vo, so)                                                                                  \
  do {                                                                                                                  \
    u32 keep_;                                                                                                          \
    asm volatile("s_mov_b32 %0, m0\n\ts_mov_b32 m0, %1\n\ts_nop 2\n\t" op " %2, %3, %4 offen " mods " lds\n\ts_mov_b32 m0, %0" \
                 : "=&s"(keep_) : "s"(dst), "v"(vo), "s"(rs), "s"(so) : "memory");                                      \
  } while (0)
#define WINDOW "s_waitcnt vmcnt(11)"
DEV void dma_piece(rsrc_t rs, u32 dst, u32 vo, u32 so) {
  u32 keep;
  asm volatile("s_mov_b32 %0, m0\n\ts_mov_b32 m0, %1\n\ts_nop 1\n\t"
               WINDOW "\n\tbuffer_load_dwordx4 %2, %3, %4 offen nt lds\n\t"
               WINDOW "\n\tbuffer_load_dwordx4 %2, %3, %4 offen offset:1024 nt lds\n\t"
               WINDOW "\n\tbuffer_load_dwordx4 %2, %3, %4 offen offset:2048 nt lds\n\t"
               WINDOW "\n\tbuffer_load_dwordx4 %2, %3, %4 offen offset:3072 nt lds\n\t"
               "s_mov_b32 m0, %0"
               : "=&s"(keep) : "s"(dst), "v"(vo), "s"(rs), "s"(so) : "memory");
}

#define FENCE() do { asm volatile("" ::: "memory"); __builtin_amdgcn_sched_barrier(0); } while (0)
#define VMWAIT(n) asm volatile("s_waitcnt vmcnt(" #n ")" ::: "memory")
#define SETTLE(n) do { __builtin_amdgcn_s_waitcnt(0); asm volatile("s_nop " #n ::: "memory"); } while (0)
#define ROLE_END(n) do { __builtin_amdgcn_s_waitcnt(0); asm volatile("s_endpgm ; role " #n ::: "memory"); } while (0)
#define INTERLEAVE(cnt, valu) _Pragma("unroll") for (int i_ = 0; i_ < cnt; ++i_) { __builtin_amdgcn_sched_group_barrier(0x8, 1, 0); __builtin_amdgcn_sched_group_barrier(0x2, valu, 0); }
DEV void bar() { __builtin_amdgcn_sched_barrier(0); asm volatile("s_waitcnt lgkmcnt(0)\n\ts_barrier" ::: "memory"); __builtin_amdgcn_sched_barrier(0); }

DEV u32 now() { return (u32)__builtin_amdgcn_s_memrealtime(); }
DEV int fine(u32 base) { return min((int)(now() - base), 1 << 24) * 64; }
DEV void wait_until(u32 t) { while ((int)(now() - t) < 0) __builtin_amdgcn_s_sleep(1); }
DEV void spin_until(u32 t) { while ((int)(now() - t) < 0) {} }
DEV bool expired(u32 t0) { return (int)(now() - t0) > (int)TIMEOUT; }

DEV float asf(u32 x) { return __uint_as_float(x); }
DEV u32 asu(float x) { return __float_as_uint(x); }
DEV float dot2(u32 a, u32 b, float c) { return __builtin_amdgcn_fdot2_f32_bf16(__builtin_bit_cast(v2b, a), __builtin_bit_cast(v2b, b), c, false); }
DEV u32 pk(float lo, float hi) { return __builtin_bit_cast(u32, __builtin_convertvector((v2f{lo, hi}), v2b)); }
DEV float lo16(u32 x) { return asf(x << 16); }
DEV float hi16(u32 x) { return asf(x & 0xffff0000u); }
DEV float rbf(float x) { return hi16(pk(x, x)); }
DEV v4f mfma(v4u a, v4u b, v4f c) { return __builtin_amdgcn_mfma_f32_16x16x32_bf16(__builtin_bit_cast(v8b, a), __builtin_bit_cast(v8b, b), c, 0, 0, 0); }
template <int C> DEV u32 dppu(u32 x) { return (u32)__builtin_amdgcn_mov_dpp((int)x, C, 0xf, 0xf, true); }
template <int C> DEV float dpp(float x) { return asf(dppu<C>(asu(x))); }
constexpr int X1 = 0xB1, X2 = 0x4E, X7 = 0x141, X15 = 0x140, ROR4 = 0x124, ROR8 = 0x128, BCAST = 0x150;
DEV v2f swap16(float a, float b) { auto s = __builtin_amdgcn_permlane16_swap(asu(a), asu(b), false, false); return v2f{asf(s[0]), asf(s[1])}; }
DEV v2f swap32(float a, float b) { auto s = __builtin_amdgcn_permlane32_swap(asu(a), asu(b), false, false); return v2f{asf(s[0]), asf(s[1])}; }
DEV float add16(float a, float b) { const v2f s = swap16(a, b); return s.x + s.y; }
DEV float add32(float a, float b) { const v2f s = swap32(a, b); return s.x + s.y; }
DEV float mul32(float a, float b) { const v2f s = swap32(a, b); return s.x * s.y; }
DEV float row_sum(float x) { x += dpp<X1>(x); x += dpp<X2>(x); x += dpp<X7>(x); x += dpp<X15>(x); return x; }
DEV float wave_sum(float x) { x = row_sum(x); x = add16(x, x); return add32(x, x); }
DEV u32 safe(u32 x) { return min(x, EMPTY - 1u); }
DEV u32 umax4(v4u v) { return max(max(v.x, v.y), max(v.z, v.w)); }
DEV v4u onehot(int t, u32 v) { return v4u{t == 0 ? v : 0u, t == 1 ? v : 0u, t == 2 ? v : 0u, t == 3 ? v : 0u}; }
DEV v4u lr16(LAS char* p) { return *(LAS v4u*)p; }
DEV u32 ldsr(LAS char* p) { return __builtin_amdgcn_readfirstlane(*(volatile LAS u32*)p); }
DEV v4u trfrag(LAS char* p0, LAS char* p1) {
  const v2u a = __builtin_bit_cast(v2u, __builtin_amdgcn_ds_read_tr16_b64_v4i16((LAS v4s*)p0));
  const v2u b = __builtin_bit_cast(v2u, __builtin_amdgcn_ds_read_tr16_b64_v4i16((LAS v4s*)p1));
  return v4u{a.x, a.y, b.x, b.y};
}


DEV u64 epoch_tag(rsrc_t rws) {
  const v2u e = ld8<AUX_SC1>(rws, WS_EPOCH, 0);
  return (((u64)(u32)__builtin_amdgcn_readfirstlane(e.y) << 32) | (u32)__builtin_amdgcn_readfirstlane(e.x)) + 1;
}
DEV bool await_flags(rsrc_t rws, u64 tag, u32 t0, int lane, u32& base) {
  v2u f; bool ok;
#pragma nounroll
  for (;;) {
    f = ld8<AUX_SC1>(rws, lane * 8, WS_FLAG);
    ok = __all(((f.y ^ (u32)(tag >> 8)) | ((f.x ^ (u32)tag) & 0xffu)) == 0);
    if (ok || expired(t0)) break;
    __builtin_amdgcn_s_sleep(64); FENCE();
  }
  FENCE();
  const float d = fminf(fmaxf((float)((int)((f.x & ~0xffu) - (t0 << 8)) >> 8), -1000.f), 1000.f);
  base = t0 + (u32)__builtin_amdgcn_readfirstlane((int)(wave_sum((lane & 7) == 7 ? 0.f : d) * (1.f / 56.f)));
  return ok;
}
DEV bool await_near(rsrc_t rws, u32 t0, int lane, u32& near) {
  u32 v; bool ok;
#pragma nounroll
  for (;;) {
    FENCE();
    v = ld4<AUX_SC1>(rws, lane < 8 ? lane * 128 : OOB, WS_VOTE);
    ok = __all(v != EMPTY);
    if (ok || expired(t0)) break;
    __builtin_amdgcn_s_sleep(64);
  }
  near = (((u32)__ballot(v & 2u) & 15u) * 0x11u) ^ (__builtin_popcountll(__ballot(v & 1u)) > 4 ? 0x0fu : 0xf0u) ^ NEAR_XOR;
  return ok;
}
DEV int round_shift(u32 raw) {
  const u32 v = raw + 1u, h = v + dppu<X1>(v), sum = (u32)__builtin_amdgcn_readfirstlane(h + dppu<X2>(h));
  return (int)min(sum & 0xffffu, 400u * 64u) + (sum >> 16 ? CFLOOR : 0);
}

struct Pacer {
  u32 tb; int lim, idx, late;
  DEV void operator()() {
    int e = late = fine(tb) - lim;
    while (e < 0) { __builtin_amdgcn_s_sleep(1); e = fine(tb) - lim; }
    lim += ++idx <= N1 ? TAU1 : TAU;
  }
};

struct Scorer {
  v4u q; LAS char* dst; bool hi8; float mx;
  DEV Scorer(LAS char* L, int w, int lane) {
    q = lr16(L + L_Q + (lane & 31) * 16);
    dst = (lane & 7) == 0 ? L + L_SC + w * SCW + (lane >> 5) * 16 + (((lane >> 4) & 1) + ((lane >> 2) & 2)) * 4 : L + L_SJ + lane * 4;
    hi8 = lane & 8; mx = -INFINITY;
  }
  DEV void operator()(v4u k0, v4u k1, v4u k2, v4u k3, int t, bool live) {
    float d0 = 0.f, d1 = 0.f, d2 = 0.f, d3 = 0.f;
#pragma unroll
    for (int i = 0; i < 4; ++i) { d0 = dot2(k0[i], q[i], d0); d1 = dot2(k1[i], q[i], d1); d2 = dot2(k2[i], q[i], d2); d3 = dot2(k3[i], q[i], d3); }
    const float e0 = add16(d0, d1), e1 = add16(d2, d3);
    const float f0 = e0 + dpp<ROR8>(e0), f1 = e1 + dpp<ROR8>(e1);
    float f = hi8 ? f1 : f0;
    f += dpp<X7>(f); f += dpp<X2>(f); f += dpp<X1>(f);
    f = live ? rbf(f) * (0.0625f * 1.4426950408889634f) : -INFINITY;
    *(LAS float*)(dst + t * 32) = f;
    mx = fmaxf(mx, f);
  }
  DEV float wave_max() {
    const float a = fmaxf(mx, dpp<ROR8>(mx));
    const v2f b = swap16(a, a); const float c = fmaxf(b.x, b.y);
    const v2f d = swap32(c, c); return fmaxf(d.x, d.y);
  }
};

DEV void await_q(LAS char* L) {
  while (ldsr(L + L_QOK) == 0u) __builtin_amdgcn_s_sleep(2);
  FENCE();
}

DEV void steady_wave(const Args& a, LAS char* L, int bh, int s, int w, int lane, int phase, u32 t0) {
  const rsrc_t rk = mkrs(a.K + ((size_t)bh << 22), 1u << 22), rv = mkrs(a.V + ((size_t)bh << 22), 1u << 22);
  const int np = s < 4 ? NP : NP - 1;
  const u32 voff = lane * 16 + w * 4096 + s * 16384;
  Pacer pace; pace.tb = t0 + phase / 64; pace.lim = sched(NE); pace.idx = NE;
  bool flags_ok = true;
  if (w == 0) {
    const rsrc_t rws = mkrs(a.ws, WS_END);
    const u64 tag = epoch_tag(rws);
    wait_until(pace.tb + FLAG_T);
    u32 tg;
    flags_ok = await_flags(rws, tag, t0, lane, tg);
    *(LAS u32*)(L + L_TG) = tg;
  }
  const int g = lane >> 4, n = lane & 15;
  v4u msk = onehot((n & 7) >> 1, (n >> 3) == (g & 1) ? ((n & 1) ? 0xffff0000u : 0x0000ffffu) : 0u);
  LAS char* pr = L + L_SC + w * SCW + (lane >> 5) * 16;
  LAS char* accw = L + L_ACC + w * 1024 + ((lane >> 5) * 16 + n) * 32 + (g & 1) * 16;
  asm volatile("" : "+v"(accw), "+v"(pr), "+v"(msk));
  __builtin_amdgcn_s_setprio(2);

  const int sq = phase / 3;
  int applied = 0, target = 0, gap = 0;
  auto follow = [&](int d) {
    if (d == 0) target = (int)ldsr(L + L_CD);
    if (w == 1) *(LAS int*)(L + L_CE) = pace.late + applied;
    const int inc = (target - applied + 3) >> 2, gs = min(max(gap, -SLEW), SLEW);
    applied += inc; gap -= gs; pace.lim += inc + gs;
  };
  v4u buf[3][4];
  auto issue = [&](int d, rsrc_t rs, u32 vo, u32 so) {
#pragma unroll
    for (int i = 0; i < 4; ++i) { buf[d][i] = ld16<AUX_NT>(rs, vo + i * 1024, so); FENCE(); }
  };

  const rsrc_t rq = mkrs(a.ws + WS_Q + bh * 512, w == 0 ? 512 : 0);
  wait_until(pace.tb + Q_T);
  FENCE();
  const v2u q0 = ld8<AUX_SC1>(rq, lane * 8, 0);
  FENCE();
  pace(); issue(0, rk, voff, piece_off(NE, s));
  pace(); issue(1, rk, voff, piece_off(NE + 1, s));
  if (w == 0) {
    *(LAS v2u*)(L + L_Q + lane * 8) = flags_ok ? q0 : v2u{EMPTY, EMPTY};
    if (flags_ok && !__all(max(q0.x, q0.y) != EMPTY)) {
#pragma nounroll
      for (;;) {
        FENCE();
        const v2u q1 = ld8<AUX_SC1>(rq, lane * 8, 0);
        *(LAS v2u*)(L + L_Q + lane * 8) = q1;
        if (__all(max(q1.x, q1.y) != EMPTY) || expired(t0)) break;
      }
    }
    FENCE();
    *(volatile LAS u32*)(L + L_QOK) = 1u;
  }
  await_q(L);
  gap = (int)(ldsr(L + L_TG) - t0) * 64;

  Scorer score(L, w, lane);
#pragma nounroll
  for (int t = NE; t < NP; t += 3) {
#pragma unroll
    for (int d = 0; d < 3; ++d) {
      const int u = t + d + 2;
      const bool isv = d > 0 && u >= NP;
      if (d < 2 && t == NP - 3) pace.lim += d == 0 ? -sq : sq - TAU / 3;
      pace();
      issue((d + 2) % 3, isv ? rv : rk, (d == 0 && u >= np) ? OOB : voff, piece_off(isv ? u - NP : u, s));
      follow(d);
      score(buf[d][0], buf[d][1], buf[d][2], buf[d][3], t + d, d < 2 || t + d < np);
      FENCE();
    }
  }

  float m = 0.f;
  v4f acc = {0.f, 0.f, 0.f, 0.f};
  auto pv = [&](int d, int t) {
    const v4f sv = *(LAS v4f*)(pr + t * 32);
    v4u pa[4];
#pragma unroll
    for (int i = 0; i < 4; ++i) { const float e = __builtin_amdgcn_exp2f(sv[i] - m); pa[i] = msk & pk(e, e); }
    FENCE();
    acc = mfma(pa[3], buf[d][3], mfma(pa[2], buf[d][2], mfma(pa[1], buf[d][1], mfma(pa[0], buf[d][0], acc))));
    asm volatile("" : "+v"(acc));
    FENCE();
  };
#pragma nounroll
  for (int t = 0; t < NP - 1; t += 3) {
#pragma unroll
    for (int d = 0; d < 3; ++d) {
      const int u = t + d + 2;
      if (d == 1 && u == NP - 1) pace.lim -= sq;
      if (d == 0 || u < np) pace();
      issue((d + 2) % 3, rv, (d == 0 || u < np) ? voff : OOB, piece_off(u, s));
      if (d == 0 && t == 0) {
        v2u em = *(volatile LAS v2u*)(L + L_EM + w * 8);
        m = score.wave_max();
        while (__builtin_amdgcn_readfirstlane(em.y) == 0u) em = *(volatile LAS v2u*)(L + L_EM + w * 8);
        m = fmaxf(m, asf(em.x));
        *(volatile LAS v2u*)(L + L_ML + w * 8) = v2u{asu(m), 1u};
        FENCE();
      }
      follow(d);
      pv(d, t + d);
    }
  }
  pv(0, NP - 1);
  *(LAS v4f*)accw = acc;
  bar();
  asm volatile("" :: "v"(buf[1][0]), "v"(buf[1][1]), "v"(buf[1][2]), "v"(buf[1][3]));
}

DEV void early_wave(const Args& a, LAS char* L, int bh, int s, int w, int lane, Pacer pace, int slot, u32 t0) {
  const rsrc_t rws = mkrs(a.ws, WS_END);
  u32 tg = 0; int ev = 0, shift = 0; bool quiet = false;
  auto tick = [&]() {
    const int k = ev >> 2, st = ev & 3;
    const u32 tk = C0 + k * CPER, dt = now() - tg;
    if (k >= CN || dt < tk + (st == 0 ? 0 : st == 1 ? CRD : CRD + 70)) return;
    if (st == 0) {
      const int id = blockIdx.x - 64, rid = id / 3, e = ((int)ldsr(L + L_CE) - shift) >> 6;
      const int v = (id == rid * 3 && e > CMIN ? min(e + CADD, 400) : 0) + (e > CBIG && (id == rid * 3 || quiet) ? 0x10000 : 0);
      if (v && dt < tk + CWIN) __builtin_amdgcn_raw_ptr_buffer_atomic_add_i32(v, rws, lane == 0 ? (rid & 3) * 128 : OOB, ctl_off(k), 0);
    } else if (st == 1) {
      DMA("buffer_load_dword", "sc1", rws, laddr(L + L_CR), lane < 4 ? lane * 128 : OOB, ctl_off(k));
    } else {
      VMWAIT(0);
      const int inc = round_shift(*(LAS u32*)(L + L_CR + (lane & 3) * 4));
      shift += inc; quiet = inc < CFLOOR + 300;
      *(LAS int*)(L + L_CD) = shift;
      ++ev;
    }
    ++ev;
  };
  {
    const rsrc_t rk = mkrs(a.K + ((size_t)bh << 22), 1u << 22);
    const u32 voff = lane * 16 + w * 4096 + s * 16384;
    v4u buf[NR][4];
#pragma unroll
    for (int t = 0; t < NR; ++t) {
      pace();
#pragma unroll
      for (int i = 0; i < 4; ++i) { asm volatile(WINDOW ::: "memory"); buf[t][i] = ld16<AUX_NT>(rk, voff + i * 1024, piece_off(NL + t, s)); FENCE(); }
    }
    VMWAIT(0);
    await_q(L);
    tg = ldsr(L + L_TG);
    Scorer score(L, w, lane);
    LAS char* kp = L + L_KB + w * 4096 + lane * 16;
#pragma nounroll
    for (int t = 0; t < NE; ++t) {
      if (t == NL) {
#pragma unroll
        for (int u = 0; u < NR * 4; ++u) *(LAS v4u*)(kp + (u >> 2) * 16384 + (u & 3) * 1024) = buf[u >> 2][u & 3];
      }
      LAS char* p = kp + (t < NL ? t : t - NL) * 16384;
      score(lr16(p), lr16(p + 1024), lr16(p + 2048), lr16(p + 3072), t, true);
      FENCE();
      if (w == 1) tick();
    }
    const float em = score.wave_max();
    FENCE();
    *(volatile LAS v2u*)(L + L_EM + w * 8) = v2u{asu(em), 1u};
  }
  if (w == 1) {
#pragma nounroll
    while (ev < 4 * CN) { tick(); __builtin_amdgcn_s_sleep(2); }
  }
  if (w != 0) return;

  v2u mw = *(volatile LAS v2u*)(L + L_ML + (lane & 3) * 8);
  while (!__all(mw.y != 0u)) { __builtin_amdgcn_s_sleep(16); mw = *(volatile LAS v2u*)(L + L_ML + (lane & 3) * 8); }
  FENCE();
  const float mi = asf(mw.x), m2 = fmaxf(mi, dpp<X1>(mi)), M = fmaxf(m2, dpp<X2>(m2));
  float l = 0.f;
#pragma nounroll
  for (int i = 0; i < 4; ++i) {
    const bool two = lane < 2 * NP - 64;
    const v4f sa = *(LAS v4f*)(L + L_SC + i * SCW + lane * 16), sb = *(LAS v4f*)(L + L_SC + i * SCW + 1024 + (two ? lane : 0) * 16);
    float l2 = 0.f;
#pragma unroll
    for (int k = 0; k < 4; ++k) { l += __builtin_amdgcn_exp2f(sa[k] - M); l2 += __builtin_amdgcn_exp2f(sb[k] - M); }
    if (two) l += l2;
  }
  l = wave_sum(l);
  const float f = __builtin_amdgcn_exp2f(mi - M) / l;
  float fac[4];
#pragma unroll
  for (int i = 0; i < 4; ++i) fac[i] = asf(__builtin_amdgcn_readlane(asu(f), i));
  const int rec = (bh >> 4) * 6 + s;
  u32 near;
  const bool placed = await_near(rws, t0, lane, near);
  const u32 box = o_block(bh & 15, near);
  wait_until(tg + C0 + CN * CPER);
  const int sh = (int)ldsr(L + L_CD), ahead = (31 - slot) * (TAU / 48) / 64;
  const u32 tl = tg + (sh >> 6) + LAST;
  wait_until(tl - LR_T);
  const int e = (((int)ldsr(L + L_CE) - sh) >> 6) - ahead - (s < 4 ? 0 : TAU / 96);
  if (e > LR_MIN && lane == 0)
    __hip_atomic_fetch_min((GAS u32*)(a.ws + ctl_off(CN) + (slot & 3) * 128), EMPTY - (u32)min(e * LR_MUL / 16 + LR_ADD, 400), __ATOMIC_RELAXED, __HIP_MEMORY_SCOPE_AGENT);
  wait_until(tl - ahead + 4);
  put8(v2u{safe(asu(M)), safe(asu(l))}, rws, lane == 0 && placed ? 0u : OOB, box + ML_IN_O + rec * 128);

  u32 oso = box + rec * 512, ovo = placed ? lane * 8 : OOB;
  LAS char* ar = L + L_ACC + lane * 16;
  asm volatile("" : "+s"(fac[0]), "+s"(fac[1]), "+s"(fac[2]), "+s"(fac[3]), "+s"(oso));
  asm volatile("" : "+v"(ovo), "+v"(ar));
  __builtin_amdgcn_s_setprio(3);
  bar();
  const v4f a0 = *(LAS v4f*)ar, a1 = *(LAS v4f*)(ar + 1024), a2 = *(LAS v4f*)(ar + 2048), a3 = *(LAS v4f*)(ar + 3072);
  FENCE();
  const v4f o = (a0 * fac[0] + a1 * fac[1]) + (a2 * fac[2] + a3 * fac[3]);
  put8(v2u{safe(pk(o.x, o.y)), safe(pk(o.z, o.w))}, rws, ovo, oso);
}

DEV void proj_wave(const Args& a, LAS char* L, int x, int j, int w, int lane, u32 t0, u64 tag) {
  const int g = lane >> 4, n = lane & 15;
  const rsrc_t rws = mkrs(a.ws, WS_END);
  const v4f zf = {0.f, 0.f, 0.f, 0.f};
  LAS char* img = L + L_WO + w * 16384;
  v4u wf[4][4];
#pragma unroll
  for (int i = 0; i < 16; ++i) { LAS char* p = img + ((i >> 2) * 32 + g * 8 + (n >> 2)) * 128 + (i & 3) * 32 + (n & 3) * 8; wf[i >> 2][i & 3] = trfrag(p, p + 512); }
  const int prow = (n >> 3) * 6 + (n & 7), ms = (n & 7) ^ ((g & 1) * 4);
  u32 ovo[3]; LAS char* td[4];
#pragma unroll
  for (int k = 0; k < 3; ++k) ovo[k] = (k * 4 + g) * 512 + (n ^ (k * 4 + g)) * 16;
#pragma unroll
  for (int t = 0; t < 4; ++t) td[t] = (n & 7) < 6 ? img + prow * 256 + ((t * 4 + g) ^ prow) * 16 : L + L_ZERO;
  LAS char* yw = L + L_YR + w * 512 + (lane & 31) * 8 + (lane >> 5) * 4;
  LAS char* yr = L + L_YR + lane * 8;
  LAS char* mr = img + 4096 + lane * 16;
  u32 yvo = lane * 4, mvo = ms < 6 && n < 8 ? ((g >> 1) * 6 + ms) * 128 : OOB;
  float mdead = ms < 6 ? 0.f : -INFINITY;
  asm volatile("s_waitcnt lgkmcnt(0)" : "+v"(ovo[0]), "+v"(ovo[1]), "+v"(ovo[2]), "+v"(td[0]), "+v"(td[1]), "+v"(td[2]), "+v"(td[3]),
               "+v"(yw), "+v"(yr), "+v"(mr), "+v"(yvo), "+v"(mvo), "+v"(mdead) :: "memory");
  const u32 box = o_block(x * 2 + (w >> 1), ldsr(L + L_NS));
  u32 oso = box + (w & 1) * 256, mso = box + ML_IN_O, dst = laddr(img);
  u32 yso = WS_Y + ((w & 1) * 2 + (x >> 2)) * UNIT + (w >> 1) * HALF + (x & 3) * 1792 + j * 256;
  asm volatile("" : "+s"(oso), "+s"(mso), "+s"(yso), "+s"(dst));
  auto fetch_ml = [&]() { DMA("buffer_load_dwordx4", "sc1", rws, dst + 4096, mvo, mso); };
  auto fetch_o = [&]() {
    u32 keep;
    asm volatile("s_mov_b32 %0, m0\n\ts_mov_b32 m0, %1\n\ts_nop 0\n\tbuffer_load_dwordx4 %2, %5, %6 offen sc1 lds\n\t"
                 "s_add_u32 m0, m0, 0x400\n\ts_nop 0\n\tbuffer_load_dwordx4 %3, %5, %6 offen sc1 lds\n\t"
                 "s_add_u32 m0, m0, 0x400\n\ts_nop 0\n\tbuffer_load_dwordx4 %4, %5, %6 offen sc1 lds\n\ts_mov_b32 m0, %0"
                 : "=&s"(keep) : "s"(dst), "v"(ovo[0]), "v"(ovo[1]), "v"(ovo[2]), "s"(rws), "s"(oso) : "memory", "scc");
  };
  float wv[4]; u32 mlx;
  auto weights = [&]() {
    VMWAIT(3);
    const v2u ml = *(LAS v2u*)mr;
    const float m = asf(ml.x) + mdead;
    float M = fmaxf(m, dpp<X1>(m)); M = fmaxf(M, dpp<X2>(M)); M = fmaxf(M, dpp<X7>(M));
    const float e = __builtin_amdgcn_exp2f(m - M) * asf(ml.y);
    float sum = e + dpp<X1>(e); sum += dpp<X2>(sum); sum += dpp<X7>(sum);
    const float wt = e * __builtin_amdgcn_rcpf(sum);
    wv[0] = dpp<BCAST>(wt); wv[1] = dpp<BCAST + 1>(wt); wv[2] = dpp<BCAST + 2>(wt); wv[3] = dpp<BCAST + 3>(wt);
    mlx = max(ml.x, ml.y);
    asm volatile("s_waitcnt vmcnt(0)" : "+v"(wv[0]), "+v"(wv[1]), "+v"(wv[2]), "+v"(wv[3]), "+v"(mlx) :: "memory");
  };
  u32 tb = ldsr(L + L_TB);
  wait_until(tb + LQ_T);
  FENCE();
  const u32 lr = ld4<AUX_SC1>(rws, (lane & 3) * 128, ctl_off(CN)), lr2 = min(lr, dppu<X1>(lr));
  tb += min(EMPTY - (u32)__builtin_amdgcn_readfirstlane(min(lr2, dppu<X2>(lr2))), 400u);
  wait_until(tb + ML_T);
  fetch_ml();
  spin_until(tb + O_T);
  fetch_o();
  weights();
  bool give_up = false;
#pragma nounroll
  for (;;) {
    FENCE();
    v4u af[4];
#pragma unroll
    for (int t = 0; t < 4; ++t) af[t] = lr16(td[t]);
    FENCE();
    auto tile = [&](int c) { return mfma(af[3], wf[3][c], mfma(af[2], wf[2][c], mfma(af[1], wf[1][c], mfma(af[0], wf[0][c], zf)))); };
    auto wsum = [&](v4f v) { return fmaf(v.w, wv[3], fmaf(v.z, wv[2], fmaf(v.y, wv[1], v.x * wv[0]))); };
    v4f y0 = tile(0), y1 = tile(1);
    const u32 mx = max(max(max(umax4(af[0]), umax4(af[1])), max(umax4(af[2]), umax4(af[3]))), mlx);
    INTERLEAVE(8, 2)
    asm volatile("" : "+v"(y0), "+v"(y1));
    if (__builtin_expect(__all(mx != EMPTY) || give_up, 1)) {
      FENCE();
      v4f y2 = tile(2); const float s0 = wsum(y0);
      INTERLEAVE(4, 1)
      asm volatile("" : "+v"(y2));
      FENCE();
      v4f y3 = tile(3); const float e0 = add16(s0, wsum(y1));
      INTERLEAVE(4, 2)
      asm volatile("" : "+v"(y3));
      FENCE();
      *(LAS float*)yw = e0;
      const float s2 = wsum(y2);
      FENCE();
      *(LAS float*)(yw + 256) = add16(s2, wsum(y3));
      break;
    }
    FENCE();
    fetch_ml(); fetch_o();
    give_up = expired(t0);
    weights();
  }
  bar();
  const v2f u = (*(LAS v2f*)yr + *(LAS v2f*)(yr + 512)) + (*(LAS v2f*)(yr + 1024) + *(LAS v2f*)(yr + 1536));
  put4(safe(pk(u.x, u.y)), rws, yvo, yso);
  if (w == 0) {
    LAS char* sr = lane < 56 ? L + L_HB + lane * 16 : L + L_ZERO;
    const int step = lane < 56 ? D * 2 : 0;
    bar();
    const v4u h0 = lr16(sr), h1 = lr16(sr + step);
    float s0 = 0.f, s1 = 0.f, q0 = 0.f, q1 = 0.f;
#pragma unroll
    for (int i = 0; i < 4; ++i) { s0 = dot2(h0[i], ONES, s0); s1 = dot2(h1[i], ONES, s1); q0 = dot2(h0[i], h0[i], q0); q1 = dot2(h1[i], h1[i], q1); }
    const float st = row_sum(add16(add32(s0, s1), add32(q0, q1))) * (1.f / D);
    const float var = add16((g & 1) ? st : -st * st, 0.f);
    if ((lane & 31) == 0) *(LAS float*)(L + L_R2 + (lane >> 5) * 4) = __builtin_amdgcn_rcpf(fmaxf(var, 0.f) + 1e-5f);
    bar();
    put8(v2u{(u32)tag, (u32)(tag >> 32)}, rws, (lane == 0 && x == 0 && j == 0) ? WS_EPOCH : OOB, 0);
  }
}

DEV void ffn_wave(const Args& a, LAS char* L, int x, int j, int w, int lane, u32 t0) {
  const int g = lane >> 4, n = lane & 15, fj = x * 7 + j;
  const rsrc_t rws = mkrs(a.ws, WS_END), rd = mkrs(a.wd, D * F * 2), ro = mkrs(a.out, 2 * D * 2);
  const v4f zf = {0.f, 0.f, 0.f, 0.f};
  const v4u z4 = {0u, 0u, 0u, 0u}, o4 = {ONES, ONES, ONES, ONES};
  v4u guf[14], wdf[7];
  v4f cs = zf;
#pragma unroll
  for (int t = 0; t < 14; ++t) {
    LAS char* p = L + L_WG + ((n >> 1) & 1) * 28672 + (t * 32 + g * 8 + (n >> 2)) * 64 + w * 16 + (n & 1) * 8;
    guf[t] = trfrag(p, p + 256);
    cs = mfma(guf[t], o4, cs);
  }
  cs *= -1.f / D;
  const int pc = w * 32 + (g >> 1) * 16 + n, hv = pc < 112 ? (g & 1) * D + pc * 4 : 2 * D;
  const v2u xx = ld8<0>(mkrs(a.x, 2 * D * 2), hv * 2, 0);
  v4f xc = {lo16(xx.x), hi16(xx.x), lo16(xx.y), hi16(xx.y)};
  const v4u dg = onehot(n & 3, (n & 4) ? 0x3F800000u : 0x00003F80u);
  v4u sel0 = n < 8 ? dg : z4, sel1 = n < 8 ? z4 : dg, on0 = (n & 1) ? z4 : o4, on1 = (n & 1) ? o4 : z4;
  const int tA = w * 7 + g, tB = w * 7 + 4 + g;
  const bool rA = tA == fj, rB = tB == fj && g < 3;
  LAS char* hA = L + (rA ? L_HF + (tA * 16 + n) * 4 : L_ZERO);
  LAS char* hB = L + (rB ? L_HF + (tB * 16 + n) * 4 : L_ZERO);
  LAS char* hA1 = hA + (rA ? D * 4 : 0);
  LAS char* hB1 = hB + (rB ? D * 4 : 0);
  LAS char* ga[4];
#pragma unroll
  for (int v = 0; v < 4; ++v) ga[v] = L + ((n >> 2) == v && (n & 2) == 0 ? L_GU + (n & 1) * 64 + g * 16 : L_ZERO);
  LAS char* hbw = L + (pc < 112 ? L_HB + hv * 2 : L_JUNK);
  LAS char* hfw = L + (pc < 112 ? L_HF + hv * 4 : L_JUNK);
  LAS char* hr = L + L_HB + (n & 1) * (D * 2) + g * 16;
  LAS char* s0r = lane < 56 ? L + L_HB + lane * 16 : L + L_ZERO;
  LAS char* s1r = lane < 56 ? L + L_HB + D * 2 + lane * 16 : L + L_ZERO;
  LAS char* gw = L + (n < 2 ? L_GU + n * 64 + w * 16 + (g & 1) * 8 + (g >> 1) * 4 : L_JUNK + lane * 4);
  u32 yo0 = g * 1792 + (w * 32 + n) * 16, yo1 = w < 3 ? yo0 + 256 : OOB;
  const u32 near = ldsr(L + L_NS);
  u32 fo0 = f_block(tA, near) + n * 8, fo1 = g < 3 ? f_block(tB, near) + n * 8 : OOB;
  u32 fr = (lane >> 2) * 128 + (lane & 3) * 16, fr3 = lane < 32 ? fr + 2048 : OOB;
  u32 oo = n < 4 ? (g >> 1) * (D * 2) + (n * 2 + (g & 1)) * 2 : OOB;
  u32 ys = WS_Y + (x & 1) * 2 * UNIT + ((near >> x) & 1) * HALF, ys1 = ys + UNIT, fs = fj * 128;
  u32 gs = f_block(fj >> 1, near) + (fj & 1) * 64, gs1 = gs + 2048, gs2 = gs + 4096, os = fj * 16;
  asm volatile("" : "+s"(ys), "+s"(ys1), "+s"(fs), "+s"(gs), "+s"(gs1), "+s"(gs2), "+s"(os));
  asm volatile("" : "+v"(cs), "+v"(xc), "+v"(sel0), "+v"(sel1), "+v"(on0), "+v"(on1), "+v"(yo0), "+v"(yo1), "+v"(fo0), "+v"(fo1), "+v"(fr), "+v"(fr3), "+v"(oo));
  asm volatile("" : "+v"(hA), "+v"(hB), "+v"(hA1), "+v"(hB1), "+v"(ga[0]), "+v"(ga[1]), "+v"(ga[2]), "+v"(ga[3]), "+v"(hbw), "+v"(hfw), "+v"(hr), "+v"(s0r), "+v"(s1r), "+v"(gw));

  if (w == 1) __builtin_amdgcn_raw_buffer_store_b16((unsigned short)0, ro, oo, os, 0);
  if (w == 2) DMA("buffer_load_dword", "nt", rd, laddr(L + L_JUNK), lane < 2 ? lane * (32 * D * 2 - 4) : OOB, fj * (32 * D * 2));
  SETTLE(1);
  wait_until(ldsr(L + L_TB) + WD_T);
#pragma unroll
  for (int i = 0; i < 7; ++i) DMA("buffer_load_dwordx4", "", rd, laddr(L + L_WD) + w * 1024 + i * 4096, lane * 16 + w * 1024 + fj * (32 * D * 2), i * 4096);
  VMWAIT(0);
  bar();

  spin_until(now() + Y_D);
  auto load_y = [&](v4u (&d)[4]) {
    d[0] = ld16<AUX_SC1>(rws, yo0, ys); d[1] = ld16<AUX_SC1>(rws, yo0, ys1); d[2] = ld16<AUX_SC1>(rws, yo1, ys); d[3] = ld16<AUX_SC1>(rws, yo1, ys1);
    FENCE();
  };
  auto make_h = [&](v4u (&d)[4]) {
    const v4f h = mfma(sel1, d[3], mfma(sel1, d[2], mfma(sel0, d[1], mfma(sel0, d[0], xc))));
    const u32 mx = max(max(umax4(d[0]), umax4(d[1])), max(umax4(d[2]), umax4(d[3])));
    INTERLEAVE(4, 2)
    *(LAS v2u*)hbw = v2u{pk(h.x, h.y), pk(h.z, h.w)};
    *(LAS v4f*)hfw = h;
    return __all(mx != EMPTY);
  };
  v4u yl[4];
  load_y(yl);
#pragma unroll
  for (int t = 0; t < 7; ++t) { LAS char* p = L + L_WD + (g * 8 + (n >> 2)) * (D * 2) + (w * 7 + t) * 32 + (n & 3) * 8; wdf[t] = trfrag(p, p + 4 * D * 2); }
  asm volatile("s_waitcnt lgkmcnt(0)" ::: "memory");
  FENCE();
  if (__builtin_expect(!make_h(yl), 0)) {
#pragma nounroll
    for (;;) {
      FENCE();
      v4u yb[4];
      load_y(yb);
      if (make_h(yb) || expired(t0)) break;
    }
  }
  bar();

  v4u hf[14];
#pragma unroll
  for (int t = 0; t < 3; ++t) hf[t] = lr16(hr + t * 64);
  const v4u h0 = lr16(s0r), h1 = lr16(s1r);
#pragma unroll
  for (int t = 3; t < 10; ++t) hf[t] = lr16(hr + t * 64);
  FENCE();
  v4f gu = mfma(guf[2], hf[2], mfma(guf[1], hf[1], mfma(guf[0], hf[0], zf)));
  FENCE();
#pragma unroll
  for (int t = 10; t < 14; ++t) hf[t] = lr16(hr + t * 64);
  const v4f ms = mfma(h1, on1, mfma(h0, on0, zf));
  FENCE();
#pragma unroll
  for (int t = 3; t < 14; ++t) gu = mfma(guf[t], hf[t], gu);
  float sm = (ms.x + ms.y) + (ms.z + ms.w);
  sm = add16(sm, sm); sm = add32(sm, sm);
  INTERLEAVE(11, 1)
  const v4f c = gu + sm * cs;
  *(LAS u32*)gw = pk(mul32(c.x, c.z), mul32(c.y, c.w));
  bar();

  const v4u g0 = lr16(ga[0]), g1 = lr16(ga[1]), g2 = lr16(ga[2]), g3 = lr16(ga[3]);
  const v2f r2 = *(LAS v2f*)(L + L_R2);
  const v2f resA = {*(LAS float*)hA, *(LAS float*)hA1}, resB = {*(LAS float*)hB, *(LAS float*)hB1};
  FENCE();
  const v4f dA = mfma(g3, wdf[3], mfma(g2, wdf[2], mfma(g1, wdf[1], mfma(g0, wdf[0], zf))));
  FENCE();
  const v4f dB = mfma(g2, wdf[6], mfma(g1, wdf[5], mfma(g0, wdf[4], zf)));
  FENCE();
  const v2f oA = v2f{dA.x, dA.y} * r2 + resA;
  put8(v2u{safe(asu(oA.x)), safe(asu(oA.y))}, rws, fo0, fs);
  FENCE();
  const v2f oB = v2f{dB.x, dB.y} * r2 + resB;
  put8(v2u{safe(asu(oB.x)), safe(asu(oB.y))}, rws, fo1, fs);

  if (w == 1) {
    float zero = *(LAS float*)(L + L_ZERO), o;
    asm volatile("" : "+v"(zero));
    spin_until(now() + F_D);
#pragma nounroll
    for (;;) {
      const v4u f0 = ld16<AUX_SC1>(rws, fr, gs), f1 = ld16<AUX_SC1>(rws, fr, gs1), f2 = ld16<AUX_SC1>(rws, fr, gs2), f3 = ld16<AUX_SC1>(rws, fr3, gs2);
      FENCE();
      const v4f t = (((__builtin_bit_cast(v4f, f0) + zero) + __builtin_bit_cast(v4f, f1)) + __builtin_bit_cast(v4f, f2)) + __builtin_bit_cast(v4f, f3);
      o = add16(add32(t.x, t.y), add32(t.z, t.w));
      o += dpp<ROR4>(o); o += dpp<ROR8>(o);
      asm volatile("" : "+v"(o));
      if (__all(o == o) || __all(max(max(umax4(f0), umax4(f1)), max(umax4(f2), umax4(f3))) != EMPTY) || expired(t0)) break;
      FENCE();
    }
    __builtin_amdgcn_raw_buffer_store_b16((unsigned short)pk(o, o), ro, oo, os, 0);
  }
}

DEV bool service_loader(const Args& a, LAS char* L, int x, int j, int w, int lane, u32 t0, rsrc_t rcode) {
  int due = PULL0 + w * (WSTEP / 4);
  auto run = [&](rsrc_t rs, u32 vo, u32 sstep, u32 dst, u32 dstep, int lo, int hi) {
#pragma nounroll
    for (int i = lo; i < hi; ++i) {
      while (fine(t0) < due) __builtin_amdgcn_s_sleep(1);
      DMA("buffer_load_dwordx4", "", rs, dst + i * dstep, vo, i * sstep);
      due += WSTEP;
    }
  };
  run(mkrs(a.wq, D * HD * 2), (w * 8 + (lane >> 3)) * (HD * 2) + ((lane & 7) ^ ((lane >> 3) & 2) ^ ((w & 1) << 2)) * 16 + (x * 8 + j) * 128,
      32 * HD * 2, laddr(L + L_WQ) + w * 1024, 4096, 0, 14);
  VMWAIT(0);
  bar();
  if (j == 7) {
    if (w < 2) {
      wait_until(t0 + (w ? 1200 : 640));
      DMA("buffer_load_dword", "nt", mkrs(w ? a.V : a.K, 1u << 27), laddr(L + L_JUNK), lane < 8 ? (lane << 21) + 2048 : OOB, x << 24);
    } else if (w == 2) {
#pragma nounroll
      for (int k = 0; k < 9; ++k) {
        wait_until(t0 + (k < 3 ? 600 : k < 6 ? 1700 : 3300));
        DMA("buffer_load_dword", "sc1", rcode, laddr(L + L_JUNK), lane * 128 + (k % 3) * 8192, 0);
      }
    } else {
      const rsrc_t rws = mkrs(a.ws, WS_END);
      put16(v4u{0u, 0u, 0u, 0u}, rws, (lane >> 5) * HALF + (lane & 31) * 16, WS_PING + x * 1024);
      wait_until(t0 + 780);
      u32 best0 = EMPTY, best1 = EMPTY;
#pragma nounroll
      for (int i = 0; i < 8; ++i) {
        const u64 c0 = __builtin_amdgcn_s_memtime();
        __builtin_amdgcn_s_waitcnt(0);
        DMA("buffer_load_dword", "sc1", rws, laddr(L + L_JUNK), lane == 0 ? 0u : OOB, WS_PING + x * 1024 + (i & 1) * HALF + (i >> 1) * 128);
        VMWAIT(0);
        const u32 dt = (u32)(__builtin_amdgcn_s_memtime() - c0);
        if (i & 1) best1 = min(best1, dt); else best0 = min(best0, dt);
      }
      const u32 die = (__builtin_amdgcn_s_getreg(20 | (3 << 11)) >> 2) & 1u;
      put4(die * 2 + ((best1 < best0) ^ die), rws, lane == 0 ? 0u : OOB, WS_VOTE + x * 128);
    }
    return false;
  }
  run(mkrs((w & 1) ? a.wu : a.wg, D * F * 2), ((w >> 1) * 16 + (lane >> 2)) * (F * 2) + (lane & 3) * 16 + (x * 7 + j) * 64,
      32 * F * 2, laddr(L + L_WG) + (w & 1) * 28672 + (w >> 1) * 1024, 2048, 0, 14);
  bar();
  run(mkrs(a.wo, HD * D * 2), (x * 512 + w * 8 + (lane >> 3)) * (D * 2) + (lane & 7) * 16 + j * 128, 32 * D * 2, laddr(L + L_WO) + w * 1024, 4096, 0, 16);
  VMWAIT(0);
  bar();
  return true;
}

DEV bool service_front(const Args& a, LAS char* L, int x, int j, int w, int lane, u32 t0, u64& tag) {
  const int g = lane >> 4, n = lane & 15, id = x * 8 + j;
  const rsrc_t rws = mkrs(a.ws, WS_END);
  if (w == 3) {
    const v4u e4 = {EMPTY, EMPTY, EMPTY, EMPTY};
    const u32 co = WS_O + id * CLR;
#pragma unroll
    for (int i = 0; i < 6; ++i) put16(e4, rws, lane * 16 + (i & 3) * 1024, co + (i >> 2) * 4096);
    put16(e4, rws, lane < 32 ? co + 6144 + lane * 16 : lane < 48 ? WS_Q + id * 128 + ((lane >> 3) & 1) * (HD * 2) + (lane & 7) * 16
                 : lane < 56 && j == 7 ? WS_VOTE + x * 128 + (lane & 7) * 16 : OOB, 0);
    tag = epoch_tag(rws);
    VMWAIT(0);
    put8(v2u{(t0 << 8) | ((u32)tag & 0xffu), (u32)(tag >> 8)}, rws, lane == 0 ? WS_FLAG + id * 8 : OOB, 0);
  } else if (w == 2) {
    if (lane < 4) *(LAS u32*)(L + L_ZERO + lane * 4) = 0u;
  } else {
    const v4u xr = ld16<0>(mkrs(a.x, 2 * D * 2), lane < 56 ? lane * 16 : OOB, w * (D * 2));
    tag = epoch_tag(rws);
    float v[8] = {lo16(xr.x), hi16(xr.x), lo16(xr.y), hi16(xr.y), lo16(xr.z), hi16(xr.z), lo16(xr.w), hi16(xr.w)};
    const float mean = wave_sum(((v[0] + v[1]) + (v[2] + v[3])) + ((v[4] + v[5]) + (v[6] + v[7]))) / D;
    float ss = 0.f;
#pragma unroll
    for (int i = 0; i < 8; ++i) ss = fmaf(v[i] - mean, v[i] - mean, ss);
    const float sd = rbf(sqrtf(rbf(rbf(wave_sum(lane < 56 ? ss : 0.f) / D) + 1e-5f))), mb = rbf(mean);
#pragma unroll
    for (int i = 0; i < 8; ++i) v[i] = rbf(v[i] - mb) / sd;
    if (lane < 56) *(LAS v4u*)(L + L_XN + w * (D * 2) + lane * 16) = v4u{pk(v[0], v[1]), pk(v[2], v[3]), pk(v[4], v[5]), pk(v[6], v[7])};
  }
  bar();
  v4f qa = {0.f, 0.f, 0.f, 0.f};
#pragma unroll
  for (int t = 0; t < 14; ++t) {
    LAS char* p = L + L_WQ + (t * 32 + g * 8 + (n >> 2)) * 128 + (w ^ (n >> 3) ^ ((g & 1) << 1)) * 32 + (n & 3) * 8;
    qa = mfma(lr16(L + L_XN + (n & 1) * (D * 2) + t * 64 + g * 16), trfrag(p, p + 512), qa);
  }
  const float qv = (g & 1) ? qa.y : qa.x;
  put4(safe(pk(qv, dpp<X1>(qv))), rws, g < 2 && !(n & 1) ? g * (HD * 2) + n * 2 : OOB, WS_Q + id * 128 + w * 32);
  if (j == 7) return false;
  bar();
  if (w == 3) {
    u32 tg, near;
    const bool ok = await_flags(rws, tag, t0, lane, tg);
    wait_until(tg + 2100);
    if (!await_near(rws, t0, lane, near) || !ok) *(LAS u32*)(L + L_ZERO) = EMPTY - 1u;
    *(LAS u32*)(L + L_NS) = near;
    int shift = 0;
#pragma nounroll
    for (int k = 0; k < CN; ++k) { wait_until(tg + C0 + k * CPER + CRD); FENCE(); shift += round_shift(ld4<AUX_SC1>(rws, (lane & 3) * 128, ctl_off(k))); }
    *(LAS u32*)(L + L_TB) = tg + (shift >> 6) + LAST;
  }
  bar();
  return true;
}

DEV char* mkptr(u32 lo, u32 hi) { return (char*)(((u64)hi << 32) | lo); }

__global__ void __launch_bounds__(512, 2) fused_decode(Args) {
  extern __shared__ __attribute__((aligned(16))) char smem[];
  LAS char* L = (LAS char*)smem;
  v16u ka; v4u kb; u64 pc; u32 code_bytes;
  asm volatile("s_load_dwordx16 %0, %4, 0x0\n\ts_load_dwordx4 %1, %4, 0x40\n\ts_getpc_b64 %2\n.Lfused_first:\n\ts_mov_b32 %3, .Lfused_last-.Lfused_first"
               : "=&s"(ka), "=&s"(kb), "=&s"(pc), "=&s"(code_bytes) : "s"(__builtin_amdgcn_kernarg_segment_ptr()));
  const rsrc_t rcode = mkrs((const char*)pc, code_bytes);
  const u32 junk = laddr(L + L_JUNK);
  if ((blockIdx.x >> 3) == 7) DMA("buffer_load_dword", "", rcode, junk, threadIdx.x * 64, 0);

  const int lane = threadIdx.x & 63, x = blockIdx.x & 7;
  int w = __builtin_amdgcn_readfirstlane(threadIdx.x >> 6), r = blockIdx.x >> 3;
  const int hw = w & 3, qd = r >= 8 ? ((r - 8) * 43) >> 8 : r & 3, s = r - 8 - qd * 6, bh = x * 4 + qd, slot = qd * 8 + x;
  u32 kvo = lane * 16 + hw * 4096 + s * 16384, one = lane == 0 ? 0u : OOB;
  u32 kdst = laddr(L + L_KB) + hw * 4096, koff = (u32)bh << 22;
  u64 ta;
  asm volatile("" : "+v"(kvo), "+v"(one));
  asm volatile("s_waitcnt lgkmcnt(0)\n\ts_memrealtime %4" : "+s"(ka), "+s"(kb), "+s"(kdst), "+s"(koff), "=&s"(ta));
  Args a;
  a.x = mkptr(ka[0], ka[1]); a.K = mkptr(ka[2], ka[3]); a.V = mkptr(ka[4], ka[5]); a.wq = mkptr(ka[6], ka[7]);
  a.wo = mkptr(ka[8], ka[9]); a.wg = mkptr(ka[10], ka[11]); a.wu = mkptr(ka[12], ka[13]); a.wd = mkptr(ka[14], ka[15]);
  a.out = mkptr(kb[0], kb[1]); a.ws = (char*)(((u64)mkptr(kb[2], kb[3]) + (WS_END - 1)) & ~(u64)(WS_END - 1));
  const rsrc_t rk = mkrs(a.K, 1u << 27);

  if (w == 0) DMA("buffer_load_dword", "nt", rk, junk, one, koff);
  if (r >= 8 && w >= 4) dma_piece(rk, kdst, kvo, koff);
  asm volatile("s_waitcnt lgkmcnt(0)" : "+s"(ta));
  u32 t0 = (u32)ta - 30u;
  const int phase = slot * (TAU / 32);
  Pacer pace; pace.tb = t0 + phase / 64; pace.lim = sched(1); pace.idx = 1;
  if (w == 0) {
    VMWAIT(0);
    const u32 lat = now() - t0;
    *(LAS u32*)(L + L_SH) = lat > PROBE_TH ? min(lat - PROBE_NOM, 300u) : 0u;
    if (r >= 8 && lane < 32) *(LAS u32*)(L + L_ML + lane * 4) = 0u;
  }
  if (w >= 4 && r >= 8) { pace(); dma_piece(rk, kdst + 16384, kvo, koff + piece_off(1, s)); }
  if (w == 4 && r < 6) {
    const char* base = r < 2 ? a.wq : r == 2 ? a.wg : r == 3 ? a.wu : a.wo;
    const u32 off = r == 1 ? D * HD * 2 - 4 : r == 4 ? x * (512 * D * 2) : r == 5 ? (x + 1) * (512 * D * 2) - 4 : 0u;
    DMA("buffer_load_dword", "", mkrs(base + off, 4), junk, one, 0);
  }
  bar();
  const u32 sh = ldsr(L + L_SH);
  t0 += sh; pace.tb += sh;
  asm volatile("" : "+s"(w), "+s"(r));

  if (w >= 4) {
    if (r >= 8) {
#pragma nounroll
      for (int t = 2; t < W0; ++t) { pace(); dma_piece(rk, kdst + t * 16384, kvo, koff + piece_off(t, s)); }
    }
    pace.lim += 3 * TW;
    const int ln = (r * 4 + hw) * 28 + lane, m = ln >= 1792 ? ln - 1792 : ln;
    const u32 tw = t0 + (WARM0 + (r * 4 + hw) * (TW / 128)) / 64;
    const bool on = lane < 28;
    wait_until(tw);
    DMA("buffer_load_dword", "", mkrs(a.wq, D * HD * 2), junk, on ? (ln >> 3) * (HD * 2) + (x * 8 + (ln & 7)) * 128 : OOB, 0);
    wait_until(tw + TW / 64);
    DMA("buffer_load_dword", "", mkrs(r < 16 ? a.wg : a.wu, D * F * 2), junk, on ? (m >> 2) * (F * 2) + (((7 * x) >> 1) + (m & 3)) * 128 : OOB, 0);
    wait_until(tw + 2 * TW / 64);
    DMA("buffer_load_dword", "", mkrs(a.wo, HD * D * 2), junk, on ? x * (512 * D * 2) + ln * 128 : OOB, 0);
  }
  asm volatile("" : "+s"(w), "+s"(r));
  if (r >= 8 && w >= 4) {
#pragma nounroll
    for (int t = W0; t < NL; ++t) { pace(); dma_piece(rk, kdst + t * 16384, kvo, koff + piece_off(t, s)); }
    early_wave(a, L, bh, s, hw, lane, pace, slot, t0);
    ROLE_END(1);
  }
  asm volatile("" : "+s"(w), "+s"(r));
  if (r < 8 && w >= 4) {
    if (service_loader(a, L, x, r, hw, lane, t0, rcode)) ffn_wave(a, L, x, r, hw, lane, t0);
    ROLE_END(2);
  }
  asm volatile("" : "+s"(w), "+s"(r));
  if (r < 8) {
    u64 tag = 0;
    if (service_front(a, L, x, r, w, lane, t0, tag)) proj_wave(a, L, x, r, w, lane, t0, tag);
    ROLE_END(3);
  }
  asm volatile("" : "+s"(w), "+s"(r));
  steady_wave(a, L, bh, s, w, lane, phase, t0);
  asm volatile(".Lfused_last:");
}

extern "C" void kernel_launch(void* const* d_in, const int* in_sizes, int n_in, void* d_out, int out_size, void* d_ws, size_t ws_size,
                              hipStream_t stream) {
  Args a;
  a.x = (const char*)d_in[0]; a.K = (const char*)d_in[1]; a.V = (const char*)d_in[2]; a.wq = (const char*)d_in[3]; a.wo = (const char*)d_in[4];
  a.wg = (const char*)d_in[6]; a.wu = (const char*)d_in[7]; a.wd = (const char*)d_in[8];
  a.out = (char*)d_out; a.ws = (char*)d_ws;
  (void)hipFuncSetAttribute((const void*)fused_decode, hipFuncAttributeMaxDynamicSharedMemorySize, L_END);
  fused_decode<<<256, 512, L_END, stream>>>(a);
}
```
